```python
import torch

def _softmax(x, axis=-1):
    return torch.softmax(x, dim=axis)

def _gelu(x):
    return torch.nn.functional.gelu(x, approximate='tanh')

def _silu(x):
    return torch.nn.functional.silu(x)

def _sigmoid(x):
    return torch.sigmoid(x)

def _layernorm(x, axis=-1, eps=1e-05):
    m = x.mean(dim=axis, keepdim=True)
    v = x.var(dim=axis, keepdim=True, unbiased=False)
    return (x - m) / torch.sqrt(v + eps)

def _scatter_add(tgt, idx, src, axis=0):
    return tgt.clone().index_add_(axis, idx, src)

def _segment_sum(src, seg, n):
    out = torch.zeros((n,) + src.shape[1:], dtype=src.dtype)
    return out.index_add_(0, seg, src)

def _pad(x, pw):
    flat = tuple((v for pair in reversed(pw) for v in pair))
    return torch.nn.functional.pad(x, flat)
kernel_name = 'sequential_preLN__linear_attn_block__mixer_gmlp__prompt'

def setup_inputs():

    _w = lambda a: (lambda t: t.to(torch.bfloat16) if t.is_floating_point() else t)(torch.as_tensor(a))

    def _A_setup():
        B, S, D = (1, 2048, 448)
        return {'x': _w((lambda *s: torch.randn(*s, dtype=torch.bfloat16) * 0.05)(B, S, D)), 'wqkv': _w((lambda *s: torch.randn(*s, dtype=torch.bfloat16) * 0.1)(D, 3 * D)), 'wo': _w((lambda *s: torch.randn(*s, dtype=torch.bfloat16) * 0.05)(D, D))}
    _A_in = _A_setup()

    def _B_setup():
        B, S, D, F = (1, 2048, 448, 1792)
        Fh = F // 2
        return {'x': _w((lambda *s: torch.randn(*s, dtype=torch.bfloat16) * 0.05)(B, S, D)), 'w_in': _w((lambda *s: torch.randn(*s, dtype=torch.bfloat16) * 0.2)(D, 2 * Fh)), 'w_sp': _w(0.01 * (lambda *s: torch.randn(*s, dtype=torch.bfloat16) * 0.2)(S, S)), 'b_sp': _w(1.0 + 0.01 * (lambda *s: torch.randn(*s, dtype=torch.bfloat16) * 0.05)(S)), 'w_out': _w((lambda *s: torch.randn(*s, dtype=torch.bfloat16) * 0.05)(Fh, D))}
    _B_in = _B_setup()
    return {'A_x': _A_in['x'], 'A_wqkv': _A_in['wqkv'], 'A_wo': _A_in['wo'], 'B_x': _B_in['x'], 'B_w_in': _B_in['w_in'], 'B_w_sp': _B_in['w_sp'], 'B_b_sp': _B_in['b_sp'], 'B_w_out': _B_in['w_out']}

def reference(A_x, A_wqkv, A_wo, B_x, B_w_in, B_w_sp, B_b_sp, B_w_out):

    def _A_ref(x, wqkv, wo):
        B, S, D = x.shape
        H = 16
        Dh = D // H
        qkv = (lambda x, *d: x.permute(*d))((x @ wqkv).reshape(B, S, 3, H, Dh), 2, 0, 3, 1, 4)
        q, k, v = (qkv[0], qkv[1], qkv[2])
        phi = lambda t: torch.where(t > 0, t + 1, torch.exp(t)) + 1e-06
        pq, pk = (phi(q), phi(k))
        kv = torch.einsum('bhsd,bhse->bhde', pk, v)
        z = torch.einsum('bhsd->bhd', pk)
        num = torch.einsum('bhqd,bhde->bhqe', pq, kv)
        den = torch.einsum('bhqd,bhd->bhq', pq, z)[..., None] + 1e-06
        o = num / den
        return (lambda x, *d: x.permute(*d))(o, 0, 2, 1, 3).reshape(B, S, D) @ wo

    def _B_ref(x, w_in, w_sp, b_sp, w_out):
        B, S, D = x.shape
        F = w_in.shape[-1]
        Fh = F // 2
        z = _gelu(torch.einsum('bsd,df->bsf', x, w_in))
        z1 = z[..., :Fh].clone()
        z2 = _layernorm(z[..., Fh:], axis=-1)
        g = torch.einsum('ts,bsf->btf', w_sp, z2) + b_sp[None, :, None]
        return torch.einsum('bsf,fd->bsd', z1 * g, w_out)
    x = A_x
    h = x + _A_ref(_layernorm(x, axis=-1), A_wqkv, A_wo)
    h2 = h + _B_ref(_layernorm(h, axis=-1), B_w_in, B_w_sp, B_b_sp, B_w_out)
    return h2
```

```cpp
#include <hip/hip_runtime.h>
#include <hip/hip_bf16.h>
#include <stdint.h>
typedef unsigned short u16;
using bf16x8 = __attribute__((ext_vector_type(8))) short;
using f32x4 = __attribute__((ext_vector_type(4))) float;
constexpr int S=2048, D=448, H=16, DH=28, F=1792, FH=896;
__device__ __forceinline__ float b2f(u16 v){ return __uint_as_float(((unsigned)v)<<16); }
__device__ __forceinline__ u16 f2b(float f){ __hip_bfloat16 b = __float2bfloat16(f); return *(u16*)&b; }

__global__ void ln_k(const u16* in, int ldi, u16* out, int ldo, int n){
  int row = blockIdx.x; int t = threadIdx.x;
  float s=0, q=0;
  for(int i=t;i<n;i+=64){ float v=b2f(in[(size_t)row*ldi+i]); s+=v; }
  for(int o=32;o;o>>=1) s+=__shfl_xor(s,o);
  float m=s/n;
  for(int i=t;i<n;i+=64){ float v=b2f(in[(size_t)row*ldi+i])-m; q+=v*v; }
  for(int o=32;o;o>>=1) q+=__shfl_xor(q,o);
  float r=1.0f/sqrtf(q/n+1e-5f);
  for(int i=t;i<n;i+=64){ out[(size_t)row*ldo+i]=f2b((b2f(in[(size_t)row*ldi+i])-m)*r); }
}
template<int EPI>
__global__ __launch_bounds__(256) void gemm_k(const u16* A,int lda,const u16* B,int ldb,u16* C,int ldc,int M,int N,int K,
   const u16* e0,int lde0,const u16* e1){
  __shared__ u16 As[64][40]; __shared__ u16 Bs[64][40];
  int bm=blockIdx.y*64, bn=blockIdx.x*64; int t=threadIdx.x, lane=t&63, w=t>>6, wr=w>>1, wc=w&1;
  f32x4 acc[2][2]={};
  for(int k0=0;k0<K;k0+=32){
    { int r=t>>2, c=(t&3)*8; *(uint4*)&As[r][c] = *(const uint4*)&A[(size_t)(bm+r)*lda+k0+c]; }
    { int kk=t>>3, c=(t&7)*8; uint4 v=*(const uint4*)&B[(size_t)(k0+kk)*ldb+bn+c]; const u16* p=(const u16*)&v; for(int j=0;j<8;j++) Bs[c+j][kk]=p[j]; }
    __syncthreads();
    bf16x8 a[2],b[2];
    for(int i=0;i<2;i++){ a[i]=*(const bf16x8*)&As[wr*32+i*16+(lane&15)][(lane>>4)*8]; b[i]=*(const bf16x8*)&Bs[wc*32+i*16+(lane&15)][(lane>>4)*8]; }
    for(int i=0;i<2;i++)for(int j=0;j<2;j++) acc[i][j]=__builtin_amdgcn_mfma_f32_16x16x32_bf16(a[i],b[j],acc[i][j],0,0,0);
    __syncthreads();
  }
  for(int i=0;i<2;i++)for(int j=0;j<2;j++)for(int r=0;r<4;r++){
    int row=bm+wr*32+i*16+(lane>>4)*4+r, col=bn+wc*32+j*16+(lane&15); float v=acc[i][j][r];
    if(EPI==1){ v += b2f(e0[(size_t)row*lde0+col]); }
    if(EPI==2){ float u=0.7978845608028654f*(v+0.044715f*v*v*v); v=0.5f*v*(1.0f+tanhf(u)); }
    if(EPI==3){ v=(v+b2f(e1[row]))*b2f(e0[(size_t)row*lde0+col]); }
    C[(size_t)row*ldc+col]=f2b(v);
  }
}
__device__ __forceinline__ float phi(float t){ return (t>0.f? t+1.f : expf(t)) + 1e-6f; }
__global__ void kv_k(const u16* qkv, float* kvp){
  int h=blockIdx.x, c=blockIdx.y, t=threadIdx.x; int d=t>>5, e=t&31;
  __shared__ float ks[128][28], vs[128][29];
  for(int i=t;i<128*28;i+=1024){ int r=i/28, j=i%28; size_t o=(size_t)(c*128+r)*1344+h*28+j; ks[r][j]=phi(b2f(qkv[o+448])); vs[r][j]=b2f(qkv[o+896]); }
  if(t<128) vs[t][28]=1.f;
  __syncthreads();
  if(d<28&&e<29){ float s=0; for(int r=0;r<128;r++) s+=ks[r][d]*vs[r][e]; kvp[((size_t)(h*16+c))*1024+d*32+e]=s; }
}
__global__ void o_k(const u16* qkv,const float* kvp,u16* o){
  int h=blockIdx.x, c=blockIdx.y, t=threadIdx.x;
  __shared__ float kv[28][32];
  for(int i=t;i<28*32;i+=256){ int d=i>>5,e=i&31; float s=0; if(e<29) for(int p=0;p<16;p++) s+=kvp[((size_t)(h*16+p))*1024+i]; kv[d][e]=s; }
  __syncthreads();
  if(t<128){ int row=c*128+t; float q[28]; float den=0;
    for(int d=0;d<28;d++){ q[d]=phi(b2f(qkv[(size_t)row*1344+h*28+d])); den+=q[d]*kv[d][28]; }
    den+=1e-6f;
    for(int e=0;e<28;e++){ float s=0; for(int d=0;d<28;d++) s+=q[d]*kv[d][e]; o[(size_t)row*448+h*28+e]=f2b(s/den); }
  }
}
extern "C" void kernel_launch(void* const* d_in, const int* in_sizes, int n_in, void* d_out, int out_size, void* d_ws, size_t ws_size, hipStream_t stream){
  const u16* x=(const u16*)d_in[0]; const u16* wqkv=(const u16*)d_in[1]; const u16* wo=(const u16*)d_in[2];
  const u16* w_in=(const u16*)d_in[4]; const u16* w_sp=(const u16*)d_in[5]; const u16* b_sp=(const u16*)d_in[6]; const u16* w_out=(const u16*)d_in[7];
  char* ws=(char*)d_ws; size_t MB=1<<20;
  u16* xn=(u16*)(ws); u16* qkv=(u16*)(ws+4*MB); float* kvp=(float*)(ws+12*MB); u16* o=(u16*)(ws+16*MB); u16* h=(u16*)(ws+20*MB);
  u16* hn=(u16*)(ws+24*MB); u16* z=(u16*)(ws+28*MB); u16* z2=(u16*)(ws+40*MB); u16* y=(u16*)(ws+48*MB); u16* out=(u16*)d_out;
  ln_k<<<S,64,0,stream>>>(x,D,xn,D,D);
  gemm_k<0><<<dim3(1344/64,S/64),256,0,stream>>>(xn,D,wqkv,1344,qkv,1344,S,1344,D,nullptr,0,nullptr);
  kv_k<<<dim3(H,16),1024,0,stream>>>(qkv,kvp);
  o_k<<<dim3(H,16),256,0,stream>>>(qkv,kvp,o);
  gemm_k<1><<<dim3(D/64,S/64),256,0,stream>>>(o,D,wo,D,h,D,S,D,D,x,D,nullptr);
  ln_k<<<S,64,0,stream>>>(h,D,hn,D,D);
  gemm_k<2><<<dim3(F/64,S/64),256,0,stream>>>(hn,D,w_in,F,z,F,S,F,D,nullptr,0,nullptr);
  ln_k<<<S,64,0,stream>>>(z+FH,F,z2,FH,FH);
  gemm_k<3><<<dim3(FH/64,S/64),256,0,stream>>>(w_sp,S,z2,FH,y,FH,S,FH,S,z,F,b_sp);
  gemm_k<1><<<dim3(D/64,S/64),256,0,stream>>>(y,FH,w_out,D,out,D,S,D,FH,h,D,nullptr);
}
```

```cpp
#include <hip/hip_runtime.h>
#include <hip/hip_bf16.h>

__global__ __launch_bounds__(256, 1) void fused_block_kernel(const void* x, const void* wqkv, void* ws, const void* wo,
                                                             const void* w_in, const void* w_sp, const void* w_out,
                                                             void* out, const void* b_sp) {
  __shared__ __attribute__((aligned(16))) char lds[163840];
  asm volatile(R"ASM(
s_mov_b64 s[0:1], %0
s_mov_b32 s2, %1
v_mov_b32 v0, %2
.Lk_start:
s_load_dwordx16 s[84:99], s[0:1], 0
s_load_dwordx2 s[44:45], s[0:1], 64
s_getpc_b64 s[4:5]
s_lshr_b32 s68, s2, 3
v_lshrrev_b32 v1, 6, v0
s_and_b32 s4, s4, 0xffffff00
s_mul_i32 s67, s68, 1280
v_lshl_add_u32 v2, v0, 7, s67
v_readfirstlane_b32 s49, v1
v_cmp_gt_u32_e32 vcc, 10, v0
v_min_u32 v2, .Lk_end-.Lk_start, v2
s_mov_b64 exec, vcc
s_cbranch_execz .Lk_nopf_1
global_load_dword v3, v2, s[4:5]
.Lk_nopf_1:
s_mov_b64 exec, -1
s_mov_b32 s48, s2
v_and_b32 v0, 63, v0
s_and_b32 s67, s48, 7
s_lshr_b32 s69, s68, 3
s_lshr_b32 s70, s67, 1
s_lshl2_add_u32 s50, s70, s69
s_and_b32 s69, s67, 1
s_and_b32 s70, s68, 7
s_lshl3_add_u32 s51, s69, s70
s_mul_i32 s69, s69, 4
s_lshr_b32 s70, s67, 1
s_mul_i32 s70, s70, 2
s_add_u32 s69, s69, s70
s_bitcmp1_b32 0x2a54, s51
s_cselect_b32 s70, 6, 0
s_add_u32 s69, s69, s70
s_mul_i32 s70, s69, 37
s_lshr_b32 s70, s70, 8
s_mul_i32 s70, s70, 7
s_sub_u32 s55, s69, s70
s_lshl2_add_u32 s69, s50, s49
s_mul_i32 s56, s69, 28672
s_mul_i32 s57, s49, 28672
s_mul_i32 s58, s49, 43008
s_mul_i32 s70, s51, 56
s_and_b32 s71, s70, 0xfffffff0
s_add_u32 s58, s58, s71
s_lshl_b32 s70, s67, 5
s_add_u32 s70, s70, s68
s_lshl_b32 s59, s70, 10
s_cmp_eq_u32 s49, 0
s_cselect_b32 s3, -1, 0
s_mov_b32 s10, 1835008
s_mov_b32 s11, 0x20000
s_mov_b32 s14, 1204224
s_mov_b32 s15, 0x20000
s_mov_b32 s18, 20971520
s_mov_b32 s19, 0x20000
v_lshrrev_b32 v48, 3, v0
v_lshrrev_b32 v49, 4, v0
v_and_b32 v50, 7, v0
v_xor_b32 v50, v50, v49
v_mul_u32_u24 v48, 896, v48
v_lshl_add_u32 v4, v50, 4, v48
v_xor_b32 v50, 4, v50
v_lshl_add_u32 v5, v50, 4, v48
v_add_u32 v5, 6144, v5
v_add_u32 v6, 12288, v4
v_add_u32 v7, 12288, v5
v_lshrrev_b32 v48, 2, v0
v_mul_u32_u24 v48, 2688, v48
v_and_b32 v49, 3, v0
v_lshl_add_u32 v8, v49, 4, v48
v_lshlrev_b32 v2, 7, v0
s_waitcnt lgkmcnt(0)
s_mov_b64 s[8:9], s[84:85]
s_mov_b64 s[12:13], s[86:87]
s_mov_b64 s[16:17], s[88:89]
s_and_b32 exec_lo, 31, s3
s_mov_b32 exec_hi, 0
buffer_load_dword v60, v2, s[16:19], s59 offen sc1
s_mov_b64 exec, -1
s_mul_i32 s7, s55, 172032
s_add_u32 s7, s7, s58
s_lshl_b32 s6, s49, 11
s_add_u32 m0, s6, 114688
s_add_u32 s6, s7, 896
buffer_load_dwordx4 v8, s[12:15], s6 offen lds
s_add_u32 s7, s7, 768
buffer_load_dwordx4 v8, s[12:15], s7 offen offset:1024 lds
s_lshl_b32 s6, s55, 7
s_add_u32 m0, s57, 0
s_add_u32 s6, s6, s56
buffer_load_dwordx4 v4, s[8:11], s6 offen lds
buffer_load_dwordx4 v5, s[8:11], s6 offen offset:1024 lds
buffer_load_dwordx4 v6, s[8:11], s6 offen offset:2048 lds
buffer_load_dwordx4 v7, s[8:11], s6 offen offset:3072 lds
s_add_u32 s55, s55, 1
s_cmp_eq_u32 s55, 7
s_cselect_b32 s55, 0, s55
s_mov_b32 s73, 0x3f803f80
v_lshlrev_b32 v1, 4, v0
v_and_b32 v48, 31, v0
v_lshrrev_b32 v49, 5, v0
v_bfe_u32 v50, v0, 1, 3
v_bfe_u32 v51, v0, 4, 1
v_xor_b32 v50, v50, v49
v_lshlrev_b32 v48, 7, v48
v_lshl_add_u32 v48, v50, 4, v48
v_add_u32 v48, s57, v48
v_bfe_u32 v50, v0, 2, 2
v_lshl_add_u32 v50, v49, 3, v50
v_lshlrev_b32 v50, 6, v50
v_lshl_add_u32 v50, v51, 5, v50
v_and_b32 v52, 3, v0
v_lshl_add_u32 v50, v52, 3, v50
v_add_u32 v50, 114688, v50
s_add_u32 s67, s49, 0
s_and_b32 s67, s67, 3
s_lshl_b32 s68, s67, 5
v_xor_b32 v9, s68, v48
v_lshl_add_u32 v13, s67, 11, v50
s_add_u32 s67, s49, 1
s_and_b32 s67, s67, 3
s_lshl_b32 s68, s67, 5
v_xor_b32 v10, s68, v48
v_lshl_add_u32 v14, s67, 11, v50
s_add_u32 s67, s49, 2
s_and_b32 s67, s67, 3
s_lshl_b32 s68, s67, 5
v_xor_b32 v11, s68, v48
v_lshl_add_u32 v15, s67, 11, v50
s_add_u32 s67, s49, 3
s_and_b32 s67, s67, 3
s_lshl_b32 s68, s67, 5
v_xor_b32 v12, s68, v48
v_lshl_add_u32 v16, s67, 11, v50
v_mov_b32 v17, 0
v_mov_b32 v18, 0
v_mov_b32 v19, 0
v_mov_b32 v21, 0
v_mov_b32 v62, 0
v_mov_b32 v63, 0
s_mov_b64 s[20:21], s[90:91]
s_mov_b64 s[24:25], s[92:93]
s_mov_b64 s[28:29], s[94:95]
s_mov_b64 s[36:37], s[96:97]
s_mov_b64 s[40:41], s[98:99]
s_mov_b32 s22, 401408
s_mov_b32 s23, 0x20000
s_mov_b32 s26, 1605632
s_mov_b32 s27, 0x20000
s_mov_b32 s30, 8388608
s_mov_b32 s31, 0x20000
s_mov_b32 s38, 802816
s_mov_b32 s39, 0x20000
s_mov_b32 s42, 1835008
s_mov_b32 s43, 0x20000
s_mov_b32 s46, 4096
s_mov_b32 s47, 0x20000
s_and_b32 s67, s48, 7
s_lshr_b32 s68, s48, 3
s_lshr_b32 s69, s67, 1
s_lshr_b32 s70, s68, 2
s_lshl3_add_u32 s52, s69, s70
s_and_b32 s69, s67, 1
s_and_b32 s70, s68, 3
s_lshl2_add_u32 s53, s69, s70
s_and_b32 s70, s68, 7
s_mul_i32 s71, s69, 7
s_add_u32 s71, s71, s70
s_add_u32 s69, s69, 14
s_cmp_eq_u32 s70, 7
s_cselect_b32 s64, s69, s71
s_cselect_b32 s65, 0, s70
s_lshr_b32 s68, s68, 3
s_lshl2_add_u32 s67, s67, s68
s_lshl2_add_u32 s54, s67, s49
s_lshl1_add_u32 s68, s67, s65
s_and_b32 s84, s68, 7
s_add_u32 s67, s67, s65
s_and_b32 s86, s67, 3
s_lshl1_add_u32 s67, s50, s64
s_mul_i32 s68, s67, 37
s_lshr_b32 s68, s68, 8
s_mul_i32 s68, s68, 7
s_sub_u32 s85, s67, s68
s_add_u32 s67, s49, s53
s_and_b32 s67, s67, 3
s_lshl_b32 s67, s67, 10
s_lshl_b32 s68, s52, 18
s_add_u32 s87, s67, s68
s_lshr_b32 s67, s53, 2
s_xor_b32 s67, s67, s52
s_and_b32 s67, s67, 1
s_lshl_b32 s88, s67, 9
s_mov_b32 s74, 0
s_mov_b32 s75, -1
s_mov_b32 s80, 0x3fb8aa3b
s_mov_b32 s82, 0x358637bd
s_mov_b32 s81, 0x3fb8aa3b
s_mov_b32 s83, 0x358637bd
v_bfe_u32 v51, v0, 4, 1
v_lshrrev_b32 v48, 3, v0
v_and_b32 v50, 7, v0
v_bfe_u32 v49, v0, 4, 2
v_lshlrev_b32 v49, 1, v49
v_xor_b32 v49, v50, v49
v_mul_u32_u24 v52, 896, v48
v_lshl_add_u32 v49, v49, 4, v52
s_lshl_b32 s67, s65, 7
v_add_u32 v32, s67, v49
v_lshlrev_b32 v49, 2, v51
v_xor_b32 v49, v50, v49
v_mul_u32_u24 v52, 3584, v48
v_lshl_add_u32 v49, v49, 4, v52
s_min_u32 s67, s64, 13
s_lshl_b32 s67, s67, 7
v_add_u32 v33, s67, v49
s_lshr_b32 s67, s51, 3
s_lshl_b32 s67, s67, 5
s_and_b32 s68, s51, 7
s_add_u32 s67, s67, s68
v_lshrrev_b32 v48, 2, v0
v_and_b32 v49, 3, v0
v_lshl_add_u32 v48, v48, 3, v49
v_lshl_add_u32 v48, v48, 3, s67
s_lshl_b32 s68, s50, 4
v_and_b32 v50, 15, v0
v_add_u32 v50, s68, v50
v_cmp_le_u32_e64 s[78:79], 16, v0
s_nop 1
v_cndmask_b32_e64 v48, v48, v50, s[78:79]
v_lshlrev_b32 v23, 10, v48
s_waitcnt vmcnt(6)
v_readlane_b32 s67, v60, 0
s_nop 0
s_and_b32 s67, s67, 1
s_xor_b32 s67, s67, 1
s_add_u32 s68, s67, 1
s_add_u32 s69, s67, 3
v_readlane_b32 s60, v60, s68
v_readlane_b32 s62, v60, s69
s_lshl_b32 s68, s68, 7
s_add_u32 s61, s59, s68
s_add_u32 s63, s61, 256
s_add_u32 s60, s60, 16
s_add_u32 s62, s62, 16
s_lshr_b32 s68, s59, 8
s_add_u32 s68, s68, s49
s_lshl_b32 s68, s68, 7
s_add_u32 s68, s68, 1048576
v_mov_b32 v48, s68
v_mov_b32 v49, 0
v_writelane_b32 v48, s59, 0
v_writelane_b32 v49, s67, 0
s_and_b32 exec_lo, 1, s3
s_mov_b32 exec_hi, 0
s_or_b32 exec_lo, exec_lo, 2
buffer_store_dword v49, v48, s[16:19], 0 offen sc1
s_mov_b64 exec, -1
s_lshl2_add_u32 s68, s50, s49
s_lshl4_add_u32 s68, s68, s64
s_lshl_b32 s68, s68, 8
s_add_u32 s68, s68, 1310720
s_and_b32 s69, s64, 1
s_mul_i32 s69, s69, 7
s_add_u32 s69, s69, s50
s_cmp_ge_u32 s50, 7
s_cselect_b32 s70, 7, 0
s_add_u32 s69, s69, s70
s_lshl_b32 s69, s69, 8
s_add_u32 s69, s69, 1703936
s_cmp_lt_u32 s50, 14
s_cselect_b32 s69, s69, s68
s_cmp_ge_u32 s64, 14
s_cselect_b32 s68, s69, s68
v_lshlrev_b32 v50, 2, v0
v_mov_b32 v51, -1
buffer_store_dword v51, v50, s[16:19], s68 offen sc1
v_mov_b32 v3, 1
s_mul_i32 s7, s55, 172032
s_add_u32 s7, s7, s58
s_lshl_b32 s6, s49, 11
s_add_u32 m0, s6, 122880
s_add_u32 s6, s7, 896
buffer_load_dwordx4 v8, s[12:15], s6 offen lds
s_add_u32 s7, s7, 768
buffer_load_dwordx4 v8, s[12:15], s7 offen offset:1024 lds
s_lshl_b32 s6, s55, 7
s_add_u32 m0, s57, 4096
s_add_u32 s6, s6, s56
buffer_load_dwordx4 v4, s[8:11], s6 offen lds
buffer_load_dwordx4 v5, s[8:11], s6 offen offset:1024 lds
buffer_load_dwordx4 v6, s[8:11], s6 offen offset:2048 lds
buffer_load_dwordx4 v7, s[8:11], s6 offen offset:3072 lds
s_add_u32 s55, s55, 1
s_cmp_eq_u32 s55, 7
s_cselect_b32 s55, 0, s55
s_waitcnt vmcnt(8)
s_barrier
ds_read_b128 v[112:115], v9 offset:0
ds_read_b64_tr_b16 v[64:65], v13 offset:0
ds_read_b64_tr_b16 v[66:67], v13 offset:256
ds_read_b64_tr_b16 v[80:81], v13 offset:1024
ds_read_b64_tr_b16 v[82:83], v13 offset:1280
ds_read_b128 v[116:119], v10 offset:0
ds_read_b64_tr_b16 v[68:69], v14 offset:0
ds_read_b64_tr_b16 v[70:71], v14 offset:256
ds_read_b64_tr_b16 v[84:85], v14 offset:1024
ds_read_b64_tr_b16 v[86:87], v14 offset:1280
s_waitcnt lgkmcnt(5)
v_mfma_f32_32x32x16_bf16 v[224:239], v[112:115], v[64:67], 0
ds_read_b128 v[120:123], v11 offset:0
ds_read_b64_tr_b16 v[72:73], v15 offset:0
ds_read_b64_tr_b16 v[74:75], v15 offset:256
ds_read_b64_tr_b16 v[88:89], v15 offset:1024
ds_read_b64_tr_b16 v[90:91], v15 offset:1280
v_mfma_f32_32x32x16_bf16 v[240:255], v[112:115], v[80:83], 0
ds_read_b128 v[124:127], v12 offset:0
ds_read_b64_tr_b16 v[76:77], v16 offset:0
ds_read_b64_tr_b16 v[78:79], v16 offset:256
ds_read_b64_tr_b16 v[92:93], v16 offset:1024
ds_read_b64_tr_b16 v[94:95], v16 offset:1280
s_waitcnt lgkmcnt(10)
v_mfma_f32_32x32x16_bf16 v[224:239], v[116:119], v[68:71], v[224:239]
s_mul_i32 s7, s55, 172032
s_add_u32 s7, s7, s58
s_lshl_b32 s6, s49, 11
s_add_u32 m0, s6, 131072
s_add_u32 s6, s7, 896
buffer_load_dwordx4 v8, s[12:15], s6 offen lds
v_mfma_f32_32x32x16_bf16 v[240:255], v[116:119], v[84:87], v[240:255]
s_add_u32 s7, s7, 768
buffer_load_dwordx4 v8, s[12:15], s7 offen offset:1024 lds
s_waitcnt lgkmcnt(5)
v_mfma_f32_32x32x16_bf16 v[224:239], v[120:123], v[72:75], v[224:239]
s_lshl_b32 s6, s55, 7
s_add_u32 m0, s57, 8192
s_add_u32 s6, s6, s56
buffer_load_dwordx4 v4, s[8:11], s6 offen lds
v_mfma_f32_32x32x16_bf16 v[240:255], v[120:123], v[88:91], v[240:255]
buffer_load_dwordx4 v5, s[8:11], s6 offen offset:1024 lds
s_waitcnt lgkmcnt(0)
v_mfma_f32_32x32x16_bf16 v[224:239], v[124:127], v[76:79], v[224:239]
buffer_load_dwordx4 v6, s[8:11], s6 offen offset:2048 lds
v_mfma_f32_32x32x16_bf16 v[240:255], v[124:127], v[92:95], v[240:255]
buffer_load_dwordx4 v7, s[8:11], s6 offen offset:3072 lds
s_add_u32 s55, s55, 1
s_cmp_eq_u32 s55, 7
s_cselect_b32 s55, 0, s55
v_dot2c_f32_bf16 v19, s73, v64
v_dot2c_f32_bf16 v21, s73, v80
v_dot2c_f32_bf16 v19, s73, v65
v_dot2c_f32_bf16 v21, s73, v81
v_dot2c_f32_bf16 v19, s73, v66
v_dot2c_f32_bf16 v21, s73, v82
v_dot2c_f32_bf16 v19, s73, v67
v_dot2c_f32_bf16 v21, s73, v83
v_dot2c_f32_bf16 v17, s73, v112
v_dot2c_f32_bf16 v18, v112, v112
v_dot2c_f32_bf16 v17, s73, v113
v_dot2c_f32_bf16 v18, v113, v113
v_dot2c_f32_bf16 v17, s73, v114
v_dot2c_f32_bf16 v18, v114, v114
v_dot2c_f32_bf16 v17, s73, v115
v_dot2c_f32_bf16 v18, v115, v115
v_dot2c_f32_bf16 v17, s73, v116
v_dot2c_f32_bf16 v18, v116, v116
v_dot2c_f32_bf16 v17, s73, v117
v_dot2c_f32_bf16 v18, v117, v117
v_dot2c_f32_bf16 v17, s73, v118
v_dot2c_f32_bf16 v18, v118, v118
v_dot2c_f32_bf16 v17, s73, v119
v_dot2c_f32_bf16 v18, v119, v119
v_dot2c_f32_bf16 v17, s73, v120
v_dot2c_f32_bf16 v18, v120, v120
v_dot2c_f32_bf16 v17, s73, v121
v_dot2c_f32_bf16 v18, v121, v121
v_dot2c_f32_bf16 v17, s73, v122
v_dot2c_f32_bf16 v18, v122, v122
v_dot2c_f32_bf16 v17, s73, v123
v_dot2c_f32_bf16 v18, v123, v123
v_dot2c_f32_bf16 v17, s73, v124
v_dot2c_f32_bf16 v18, v124, v124
v_dot2c_f32_bf16 v17, s73, v125
v_dot2c_f32_bf16 v18, v125, v125
v_dot2c_f32_bf16 v17, s73, v126
v_dot2c_f32_bf16 v18, v126, v126
v_dot2c_f32_bf16 v17, s73, v127
v_dot2c_f32_bf16 v18, v127, v127
s_waitcnt vmcnt(6)
s_barrier
ds_read_b128 v[128:131], v9 offset:4096
ds_read_b64_tr_b16 v[64:65], v13 offset:8192
ds_read_b64_tr_b16 v[66:67], v13 offset:8448
ds_read_b64_tr_b16 v[80:81], v13 offset:9216
ds_read_b64_tr_b16 v[82:83], v13 offset:9472
ds_read_b128 v[132:135], v10 offset:4096
ds_read_b64_tr_b16 v[68:69], v14 offset:8192
ds_read_b64_tr_b16 v[70:71], v14 offset:8448
ds_read_b64_tr_b16 v[84:85], v14 offset:9216
ds_read_b64_tr_b16 v[86:87], v14 offset:9472
s_waitcnt lgkmcnt(5)
v_mfma_f32_32x32x16_bf16 v[224:239], v[128:131], v[64:67], v[224:239]
ds_read_b128 v[136:139], v11 offset:4096
ds_read_b64_tr_b16 v[72:73], v15 offset:8192
ds_read_b64_tr_b16 v[74:75], v15 offset:8448
ds_read_b64_tr_b16 v[88:89], v15 offset:9216
ds_read_b64_tr_b16 v[90:91], v15 offset:9472
v_mfma_f32_32x32x16_bf16 v[240:255], v[128:131], v[80:83], v[240:255]
ds_read_b128 v[140:143], v12 offset:4096
ds_read_b64_tr_b16 v[76:77], v16 offset:8192
ds_read_b64_tr_b16 v[78:79], v16 offset:8448
ds_read_b64_tr_b16 v[92:93], v16 offset:9216
ds_read_b64_tr_b16 v[94:95], v16 offset:9472
s_waitcnt lgkmcnt(10)
v_mfma_f32_32x32x16_bf16 v[224:239], v[132:135], v[68:71], v[224:239]
s_mul_i32 s7, s55, 172032
s_add_u32 s7, s7, s58
s_lshl_b32 s6, s49, 11
s_add_u32 m0, s6, 139264
s_add_u32 s6, s7, 896
buffer_load_dwordx4 v8, s[12:15], s6 offen lds
v_mfma_f32_32x32x16_bf16 v[240:255], v[132:135], v[84:87], v[240:255]
s_add_u32 s7, s7, 768
buffer_load_dwordx4 v8, s[12:15], s7 offen offset:1024 lds
s_waitcnt lgkmcnt(5)
v_mfma_f32_32x32x16_bf16 v[224:239], v[136:139], v[72:75], v[224:239]
s_lshl_b32 s6, s55, 7
s_add_u32 m0, s57, 12288
s_add_u32 s6, s6, s56
buffer_load_dwordx4 v4, s[8:11], s6 offen lds
v_mfma_f32_32x32x16_bf16 v[240:255], v[136:139], v[88:91], v[240:255]
buffer_load_dwordx4 v5, s[8:11], s6 offen offset:1024 lds
s_waitcnt lgkmcnt(0)
v_mfma_f32_32x32x16_bf16 v[224:239], v[140:143], v[76:79], v[224:239]
buffer_load_dwordx4 v6, s[8:11], s6 offen offset:2048 lds
v_mfma_f32_32x32x16_bf16 v[240:255], v[140:143], v[92:95], v[240:255]
buffer_load_dwordx4 v7, s[8:11], s6 offen offset:3072 lds
s_add_u32 s55, s55, 1
s_cmp_eq_u32 s55, 7
s_cselect_b32 s55, 0, s55
v_dot2c_f32_bf16 v19, s73, v64
v_dot2c_f32_bf16 v21, s73, v80
v_dot2c_f32_bf16 v19, s73, v65
v_dot2c_f32_bf16 v21, s73, v81
v_dot2c_f32_bf16 v19, s73, v66
v_dot2c_f32_bf16 v21, s73, v82
v_dot2c_f32_bf16 v19, s73, v67
v_dot2c_f32_bf16 v21, s73, v83
v_dot2c_f32_bf16 v17, s73, v128
v_dot2c_f32_bf16 v18, v128, v128
v_dot2c_f32_bf16 v17, s73, v129
v_dot2c_f32_bf16 v18, v129, v129
v_dot2c_f32_bf16 v17, s73, v130
v_dot2c_f32_bf16 v18, v130, v130
v_dot2c_f32_bf16 v17, s73, v131
v_dot2c_f32_bf16 v18, v131, v131
v_dot2c_f32_bf16 v17, s73, v132
v_dot2c_f32_bf16 v18, v132, v132
v_dot2c_f32_bf16 v17, s73, v133
v_dot2c_f32_bf16 v18, v133, v133
v_dot2c_f32_bf16 v17, s73, v134
v_dot2c_f32_bf16 v18, v134, v134
v_dot2c_f32_bf16 v17, s73, v135
v_dot2c_f32_bf16 v18, v135, v135
v_dot2c_f32_bf16 v17, s73, v136
v_dot2c_f32_bf16 v18, v136, v136
v_dot2c_f32_bf16 v17, s73, v137
v_dot2c_f32_bf16 v18, v137, v137
v_dot2c_f32_bf16 v17, s73, v138
v_dot2c_f32_bf16 v18, v138, v138
v_dot2c_f32_bf16 v17, s73, v139
v_dot2c_f32_bf16 v18, v139, v139
v_dot2c_f32_bf16 v17, s73, v140
v_dot2c_f32_bf16 v18, v140, v140
v_dot2c_f32_bf16 v17, s73, v141
v_dot2c_f32_bf16 v18, v141, v141
v_dot2c_f32_bf16 v17, s73, v142
v_dot2c_f32_bf16 v18, v142, v142
v_dot2c_f32_bf16 v17, s73, v143
v_dot2c_f32_bf16 v18, v143, v143
s_waitcnt vmcnt(6)
s_barrier
ds_read_b128 v[144:147], v9 offset:8192
ds_read_b64_tr_b16 v[64:65], v13 offset:16384
ds_read_b64_tr_b16 v[66:67], v13 offset:16640
ds_read_b64_tr_b16 v[80:81], v13 offset:17408
ds_read_b64_tr_b16 v[82:83], v13 offset:17664
ds_read_b128 v[148:151], v10 offset:8192
ds_read_b64_tr_b16 v[68:69], v14 offset:16384
ds_read_b64_tr_b16 v[70:71], v14 offset:16640
ds_read_b64_tr_b16 v[84:85], v14 offset:17408
ds_read_b64_tr_b16 v[86:87], v14 offset:17664
s_waitcnt lgkmcnt(5)
v_mfma_f32_32x32x16_bf16 v[224:239], v[144:147], v[64:67], v[224:239]
ds_read_b128 v[152:155], v11 offset:8192
ds_read_b64_tr_b16 v[72:73], v15 offset:16384
ds_read_b64_tr_b16 v[74:75], v15 offset:16640
ds_read_b64_tr_b16 v[88:89], v15 offset:17408
ds_read_b64_tr_b16 v[90:91], v15 offset:17664
v_mfma_f32_32x32x16_bf16 v[240:255], v[144:147], v[80:83], v[240:255]
ds_read_b128 v[156:159], v12 offset:8192
ds_read_b64_tr_b16 v[76:77], v16 offset:16384
ds_read_b64_tr_b16 v[78:79], v16 offset:16640
ds_read_b64_tr_b16 v[92:93], v16 offset:17408
ds_read_b64_tr_b16 v[94:95], v16 offset:17664
s_waitcnt lgkmcnt(10)
v_mfma_f32_32x32x16_bf16 v[224:239], v[148:151], v[68:71], v[224:239]
s_mul_i32 s7, s55, 172032
s_add_u32 s7, s7, s58
s_lshl_b32 s6, s49, 11
s_add_u32 m0, s6, 147456
s_add_u32 s6, s7, 896
buffer_load_dwordx4 v8, s[12:15], s6 offen lds
v_mfma_f32_32x32x16_bf16 v[240:255], v[148:151], v[84:87], v[240:255]
s_add_u32 s7, s7, 768
buffer_load_dwordx4 v8, s[12:15], s7 offen offset:1024 lds
s_waitcnt lgkmcnt(5)
v_mfma_f32_32x32x16_bf16 v[224:239], v[152:155], v[72:75], v[224:239]
s_lshl_b32 s6, s55, 7
s_add_u32 m0, s57, 16384
s_add_u32 s6, s6, s56
buffer_load_dwordx4 v4, s[8:11], s6 offen lds
v_mfma_f32_32x32x16_bf16 v[240:255], v[152:155], v[88:91], v[240:255]
buffer_load_dwordx4 v5, s[8:11], s6 offen offset:1024 lds
s_waitcnt lgkmcnt(0)
v_mfma_f32_32x32x16_bf16 v[224:239], v[156:159], v[76:79], v[224:239]
buffer_load_dwordx4 v6, s[8:11], s6 offen offset:2048 lds
v_mfma_f32_32x32x16_bf16 v[240:255], v[156:159], v[92:95], v[240:255]
buffer_load_dwordx4 v7, s[8:11], s6 offen offset:3072 lds
s_add_u32 s55, s55, 1
s_cmp_eq_u32 s55, 7
s_cselect_b32 s55, 0, s55
v_dot2c_f32_bf16 v19, s73, v64
v_dot2c_f32_bf16 v21, s73, v80
v_dot2c_f32_bf16 v19, s73, v65
v_dot2c_f32_bf16 v21, s73, v81
v_dot2c_f32_bf16 v19, s73, v66
v_dot2c_f32_bf16 v21, s73, v82
v_dot2c_f32_bf16 v19, s73, v67
v_dot2c_f32_bf16 v21, s73, v83
v_dot2c_f32_bf16 v17, s73, v144
v_dot2c_f32_bf16 v18, v144, v144
v_dot2c_f32_bf16 v17, s73, v145
v_dot2c_f32_bf16 v18, v145, v145
v_dot2c_f32_bf16 v17, s73, v146
v_dot2c_f32_bf16 v18, v146, v146
v_dot2c_f32_bf16 v17, s73, v147
v_dot2c_f32_bf16 v18, v147, v147
v_dot2c_f32_bf16 v17, s73, v148
v_dot2c_f32_bf16 v18, v148, v148
v_dot2c_f32_bf16 v17, s73, v149
v_dot2c_f32_bf16 v18, v149, v149
v_dot2c_f32_bf16 v17, s73, v150
v_dot2c_f32_bf16 v18, v150, v150
v_dot2c_f32_bf16 v17, s73, v151
v_dot2c_f32_bf16 v18, v151, v151
v_dot2c_f32_bf16 v17, s73, v152
v_dot2c_f32_bf16 v18, v152, v152
v_dot2c_f32_bf16 v17, s73, v153
v_dot2c_f32_bf16 v18, v153, v153
v_dot2c_f32_bf16 v17, s73, v154
v_dot2c_f32_bf16 v18, v154, v154
v_dot2c_f32_bf16 v17, s73, v155
v_dot2c_f32_bf16 v18, v155, v155
v_dot2c_f32_bf16 v17, s73, v156
v_dot2c_f32_bf16 v18, v156, v156
v_dot2c_f32_bf16 v17, s73, v157
v_dot2c_f32_bf16 v18, v157, v157
v_dot2c_f32_bf16 v17, s73, v158
v_dot2c_f32_bf16 v18, v158, v158
v_dot2c_f32_bf16 v17, s73, v159
v_dot2c_f32_bf16 v18, v159, v159
s_waitcnt vmcnt(6)
s_barrier
ds_read_b128 v[160:163], v9 offset:12288
ds_read_b64_tr_b16 v[64:65], v13 offset:24576
ds_read_b64_tr_b16 v[66:67], v13 offset:24832
ds_read_b64_tr_b16 v[80:81], v13 offset:25600
ds_read_b64_tr_b16 v[82:83], v13 offset:25856
ds_read_b128 v[164:167], v10 offset:12288
ds_read_b64_tr_b16 v[68:69], v14 offset:24576
ds_read_b64_tr_b16 v[70:71], v14 offset:24832
ds_read_b64_tr_b16 v[84:85], v14 offset:25600
ds_read_b64_tr_b16 v[86:87], v14 offset:25856
s_waitcnt lgkmcnt(5)
v_mfma_f32_32x32x16_bf16 v[224:239], v[160:163], v[64:67], v[224:239]
ds_read_b128 v[168:171], v11 offset:12288
ds_read_b64_tr_b16 v[72:73], v15 offset:24576
ds_read_b64_tr_b16 v[74:75], v15 offset:24832
ds_read_b64_tr_b16 v[88:89], v15 offset:25600
ds_read_b64_tr_b16 v[90:91], v15 offset:25856
v_mfma_f32_32x32x16_bf16 v[240:255], v[160:163], v[80:83], v[240:255]
ds_read_b128 v[172:175], v12 offset:12288
ds_read_b64_tr_b16 v[76:77], v16 offset:24576
ds_read_b64_tr_b16 v[78:79], v16 offset:24832
ds_read_b64_tr_b16 v[92:93], v16 offset:25600
ds_read_b64_tr_b16 v[94:95], v16 offset:25856
s_waitcnt lgkmcnt(10)
v_mfma_f32_32x32x16_bf16 v[224:239], v[164:167], v[68:71], v[224:239]
s_mul_i32 s7, s55, 172032
s_add_u32 s7, s7, s58
s_lshl_b32 s6, s49, 11
s_add_u32 m0, s6, 114688
s_add_u32 s6, s7, 896
buffer_load_dwordx4 v8, s[12:15], s6 offen lds
v_mfma_f32_32x32x16_bf16 v[240:255], v[164:167], v[84:87], v[240:255]
s_add_u32 s7, s7, 768
buffer_load_dwordx4 v8, s[12:15], s7 offen offset:1024 lds
s_waitcnt lgkmcnt(5)
v_mfma_f32_32x32x16_bf16 v[224:239], v[168:171], v[72:75], v[224:239]
s_lshl_b32 s6, s55, 7
s_add_u32 m0, s57, 20480
s_add_u32 s6, s6, s56
buffer_load_dwordx4 v4, s[8:11], s6 offen lds
v_mfma_f32_32x32x16_bf16 v[240:255], v[168:171], v[88:91], v[240:255]
buffer_load_dwordx4 v5, s[8:11], s6 offen offset:1024 lds
s_waitcnt lgkmcnt(0)
v_mfma_f32_32x32x16_bf16 v[224:239], v[172:175], v[76:79], v[224:239]
buffer_load_dwordx4 v6, s[8:11], s6 offen offset:2048 lds
v_mfma_f32_32x32x16_bf16 v[240:255], v[172:175], v[92:95], v[240:255]
buffer_load_dwordx4 v7, s[8:11], s6 offen offset:3072 lds
s_add_u32 s55, s55, 1
s_cmp_eq_u32 s55, 7
s_cselect_b32 s55, 0, s55
v_dot2c_f32_bf16 v19, s73, v64
v_dot2c_f32_bf16 v21, s73, v80
v_dot2c_f32_bf16 v19, s73, v65
v_dot2c_f32_bf16 v21, s73, v81
v_dot2c_f32_bf16 v19, s73, v66
v_dot2c_f32_bf16 v21, s73, v82
v_dot2c_f32_bf16 v19, s73, v67
v_dot2c_f32_bf16 v21, s73, v83
v_dot2c_f32_bf16 v17, s73, v160
v_dot2c_f32_bf16 v18, v160, v160
v_dot2c_f32_bf16 v17, s73, v161
v_dot2c_f32_bf16 v18, v161, v161
v_dot2c_f32_bf16 v17, s73, v162
v_dot2c_f32_bf16 v18, v162, v162
v_dot2c_f32_bf16 v17, s73, v163
v_dot2c_f32_bf16 v18, v163, v163
v_dot2c_f32_bf16 v17, s73, v164
v_dot2c_f32_bf16 v18, v164, v164
v_dot2c_f32_bf16 v17, s73, v165
v_dot2c_f32_bf16 v18, v165, v165
v_dot2c_f32_bf16 v17, s73, v166
v_dot2c_f32_bf16 v18, v166, v166
v_dot2c_f32_bf16 v17, s73, v167
v_dot2c_f32_bf16 v18, v167, v167
v_dot2c_f32_bf16 v17, s73, v168
v_dot2c_f32_bf16 v18, v168, v168
v_dot2c_f32_bf16 v17, s73, v169
v_dot2c_f32_bf16 v18, v169, v169
v_dot2c_f32_bf16 v17, s73, v170
v_dot2c_f32_bf16 v18, v170, v170
v_dot2c_f32_bf16 v17, s73, v171
v_dot2c_f32_bf16 v18, v171, v171
v_dot2c_f32_bf16 v17, s73, v172
v_dot2c_f32_bf16 v18, v172, v172
v_dot2c_f32_bf16 v17, s73, v173
v_dot2c_f32_bf16 v18, v173, v173
v_dot2c_f32_bf16 v17, s73, v174
v_dot2c_f32_bf16 v18, v174, v174
v_dot2c_f32_bf16 v17, s73, v175
v_dot2c_f32_bf16 v18, v175, v175
s_waitcnt vmcnt(6)
s_barrier
ds_read_b128 v[176:179], v9 offset:16384
ds_read_b64_tr_b16 v[64:65], v13 offset:32768
ds_read_b64_tr_b16 v[66:67], v13 offset:33024
ds_read_b64_tr_b16 v[80:81], v13 offset:33792
ds_read_b64_tr_b16 v[82:83], v13 offset:34048
ds_read_b128 v[180:183], v10 offset:16384
ds_read_b64_tr_b16 v[68:69], v14 offset:32768
ds_read_b64_tr_b16 v[70:71], v14 offset:33024
ds_read_b64_tr_b16 v[84:85], v14 offset:33792
ds_read_b64_tr_b16 v[86:87], v14 offset:34048
s_waitcnt lgkmcnt(5)
v_mfma_f32_32x32x16_bf16 v[224:239], v[176:179], v[64:67], v[224:239]
ds_read_b128 v[184:187], v11 offset:16384
ds_read_b64_tr_b16 v[72:73], v15 offset:32768
ds_read_b64_tr_b16 v[74:75], v15 offset:33024
ds_read_b64_tr_b16 v[88:89], v15 offset:33792
ds_read_b64_tr_b16 v[90:91], v15 offset:34048
v_mfma_f32_32x32x16_bf16 v[240:255], v[176:179], v[80:83], v[240:255]
ds_read_b128 v[188:191], v12 offset:16384
ds_read_b64_tr_b16 v[76:77], v16 offset:32768
ds_read_b64_tr_b16 v[78:79], v16 offset:33024
ds_read_b64_tr_b16 v[92:93], v16 offset:33792
ds_read_b64_tr_b16 v[94:95], v16 offset:34048
s_waitcnt lgkmcnt(10)
v_mfma_f32_32x32x16_bf16 v[224:239], v[180:183], v[68:71], v[224:239]
s_lshl_b32 s6, s55, 7
s_add_u32 m0, s57, 24576
s_add_u32 s6, s6, s56
buffer_load_dwordx4 v4, s[8:11], s6 offen lds
v_mfma_f32_32x32x16_bf16 v[240:255], v[180:183], v[84:87], v[240:255]
buffer_load_dwordx4 v5, s[8:11], s6 offen offset:1024 lds
s_waitcnt lgkmcnt(5)
v_mfma_f32_32x32x16_bf16 v[224:239], v[184:187], v[72:75], v[224:239]
buffer_load_dwordx4 v6, s[8:11], s6 offen offset:2048 lds
v_mfma_f32_32x32x16_bf16 v[240:255], v[184:187], v[88:91], v[240:255]
buffer_load_dwordx4 v7, s[8:11], s6 offen offset:3072 lds
s_waitcnt lgkmcnt(0)
v_mfma_f32_32x32x16_bf16 v[224:239], v[188:191], v[76:79], v[224:239]
s_mul_i32 s7, s55, 172032
s_add_u32 s7, s7, s58
s_lshl_b32 s6, s49, 11
s_add_u32 m0, s6, 122880
s_add_u32 s6, s7, 896
buffer_load_dwordx4 v8, s[12:15], s6 offen lds
v_mfma_f32_32x32x16_bf16 v[240:255], v[188:191], v[92:95], v[240:255]
s_add_u32 s7, s7, 768
buffer_load_dwordx4 v8, s[12:15], s7 offen offset:1024 lds
s_add_u32 s55, s55, 1
s_cmp_eq_u32 s55, 7
s_cselect_b32 s55, 0, s55
v_dot2c_f32_bf16 v19, s73, v64
v_dot2c_f32_bf16 v21, s73, v80
v_dot2c_f32_bf16 v19, s73, v65
v_dot2c_f32_bf16 v21, s73, v81
v_dot2c_f32_bf16 v19, s73, v66
v_dot2c_f32_bf16 v21, s73, v82
v_dot2c_f32_bf16 v19, s73, v67
v_dot2c_f32_bf16 v21, s73, v83
v_dot2c_f32_bf16 v17, s73, v176
v_dot2c_f32_bf16 v18, v176, v176
v_dot2c_f32_bf16 v17, s73, v177
v_dot2c_f32_bf16 v18, v177, v177
v_dot2c_f32_bf16 v17, s73, v178
v_dot2c_f32_bf16 v18, v178, v178
v_dot2c_f32_bf16 v17, s73, v179
v_dot2c_f32_bf16 v18, v179, v179
v_dot2c_f32_bf16 v17, s73, v180
v_dot2c_f32_bf16 v18, v180, v180
v_dot2c_f32_bf16 v17, s73, v181
v_dot2c_f32_bf16 v18, v181, v181
v_dot2c_f32_bf16 v17, s73, v182
v_dot2c_f32_bf16 v18, v182, v182
v_dot2c_f32_bf16 v17, s73, v183
v_dot2c_f32_bf16 v18, v183, v183
v_dot2c_f32_bf16 v17, s73, v184
v_dot2c_f32_bf16 v18, v184, v184
v_dot2c_f32_bf16 v17, s73, v185
v_dot2c_f32_bf16 v18, v185, v185
v_dot2c_f32_bf16 v17, s73, v186
v_dot2c_f32_bf16 v18, v186, v186
v_dot2c_f32_bf16 v17, s73, v187
v_dot2c_f32_bf16 v18, v187, v187
v_dot2c_f32_bf16 v17, s73, v188
v_dot2c_f32_bf16 v18, v188, v188
v_dot2c_f32_bf16 v17, s73, v189
v_dot2c_f32_bf16 v18, v189, v189
v_dot2c_f32_bf16 v17, s73, v190
v_dot2c_f32_bf16 v18, v190, v190
v_dot2c_f32_bf16 v17, s73, v191
v_dot2c_f32_bf16 v18, v191, v191
s_waitcnt vmcnt(6)
s_barrier
ds_read_b128 v[192:195], v9 offset:20480
ds_read_b64_tr_b16 v[64:65], v13 offset:0
ds_read_b64_tr_b16 v[66:67], v13 offset:256
ds_read_b64_tr_b16 v[80:81], v13 offset:1024
ds_read_b64_tr_b16 v[82:83], v13 offset:1280
ds_read_b128 v[196:199], v10 offset:20480
ds_read_b64_tr_b16 v[68:69], v14 offset:0
ds_read_b64_tr_b16 v[70:71], v14 offset:256
ds_read_b64_tr_b16 v[84:85], v14 offset:1024
ds_read_b64_tr_b16 v[86:87], v14 offset:1280
s_waitcnt lgkmcnt(5)
v_mfma_f32_32x32x16_bf16 v[224:239], v[192:195], v[64:67], v[224:239]
ds_read_b128 v[200:203], v11 offset:20480
ds_read_b64_tr_b16 v[72:73], v15 offset:0
ds_read_b64_tr_b16 v[74:75], v15 offset:256
ds_read_b64_tr_b16 v[88:89], v15 offset:1024
ds_read_b64_tr_b16 v[90:91], v15 offset:1280
v_mfma_f32_32x32x16_bf16 v[240:255], v[192:195], v[80:83], v[240:255]
ds_read_b128 v[204:207], v12 offset:20480
ds_read_b64_tr_b16 v[76:77], v16 offset:0
ds_read_b64_tr_b16 v[78:79], v16 offset:256
ds_read_b64_tr_b16 v[92:93], v16 offset:1024
ds_read_b64_tr_b16 v[94:95], v16 offset:1280
s_waitcnt lgkmcnt(10)
v_mfma_f32_32x32x16_bf16 v[224:239], v[196:199], v[68:71], v[224:239]
s_lshl_b32 s6, s49, 10
s_add_u32 m0, s6, 131072
s_mul_i32 s7, s55, 172032
s_add_u32 s7, s7, s58
buffer_load_dwordx4 v8, s[12:15], s7 offen lds
s_add_u32 s55, s55, 1
s_cmp_eq_u32 s55, 7
s_cselect_b32 s55, 0, s55
v_mfma_f32_32x32x16_bf16 v[240:255], v[196:199], v[84:87], v[240:255]
s_lshl_b32 s6, s49, 10
s_add_u32 m0, s6, 135168
s_mul_i32 s7, s55, 172032
s_add_u32 s7, s7, s58
buffer_load_dwordx4 v8, s[12:15], s7 offen lds
s_add_u32 s55, s55, 1
s_cmp_eq_u32 s55, 7
s_cselect_b32 s55, 0, s55
s_waitcnt lgkmcnt(5)
v_mfma_f32_32x32x16_bf16 v[224:239], v[200:203], v[72:75], v[224:239]
s_lshl_b32 s6, s49, 10
s_add_u32 m0, s6, 139264
s_mul_i32 s7, s55, 172032
s_add_u32 s7, s7, s58
buffer_load_dwordx4 v8, s[12:15], s7 offen lds
s_add_u32 s55, s55, 1
s_cmp_eq_u32 s55, 7
s_cselect_b32 s55, 0, s55
v_mfma_f32_32x32x16_bf16 v[240:255], v[200:203], v[88:91], v[240:255]
s_lshl_b32 s6, s49, 10
s_add_u32 m0, s6, 143360
s_mul_i32 s7, s55, 172032
s_add_u32 s7, s7, s58
buffer_load_dwordx4 v8, s[12:15], s7 offen lds
s_add_u32 s55, s55, 1
s_cmp_eq_u32 s55, 7
s_cselect_b32 s55, 0, s55
s_lshl_b32 s6, s49, 10
s_add_u32 m0, s6, 147456
s_mul_i32 s7, s55, 172032
s_add_u32 s7, s7, s58
buffer_load_dwordx4 v8, s[12:15], s7 offen lds
s_add_u32 s55, s55, 1
s_cmp_eq_u32 s55, 7
s_cselect_b32 s55, 0, s55
s_waitcnt lgkmcnt(0)
v_mfma_f32_32x32x16_bf16 v[224:239], v[204:207], v[76:79], v[224:239]
s_lshl_b32 s6, s49, 10
s_add_u32 m0, s6, 151552
s_mul_i32 s7, s55, 172032
s_add_u32 s7, s7, s58
buffer_load_dwordx4 v8, s[12:15], s7 offen lds
s_add_u32 s55, s55, 1
s_cmp_eq_u32 s55, 7
s_cselect_b32 s55, 0, s55
v_mfma_f32_32x32x16_bf16 v[240:255], v[204:207], v[92:95], v[240:255]
s_lshl_b32 s6, s49, 10
s_add_u32 m0, s6, 155648
s_mul_i32 s7, s55, 172032
s_add_u32 s7, s7, s58
buffer_load_dwordx4 v8, s[12:15], s7 offen lds
s_add_u32 s55, s55, 1
s_cmp_eq_u32 s55, 7
s_cselect_b32 s55, 0, s55
s_waitcnt vmcnt(9)
ds_read_b128 v[208:211], v9 offset:24576
ds_read_b128 v[212:215], v10 offset:24576
ds_read_b128 v[216:219], v11 offset:24576
ds_read_b128 v[220:223], v12 offset:24576
v_dot2c_f32_bf16 v19, s73, v64
v_dot2c_f32_bf16 v21, s73, v80
v_dot2c_f32_bf16 v19, s73, v65
v_dot2c_f32_bf16 v21, s73, v81
v_dot2c_f32_bf16 v19, s73, v66
v_dot2c_f32_bf16 v21, s73, v82
v_dot2c_f32_bf16 v19, s73, v67
v_dot2c_f32_bf16 v21, s73, v83
v_dot2c_f32_bf16 v17, s73, v192
v_dot2c_f32_bf16 v18, v192, v192
v_dot2c_f32_bf16 v17, s73, v193
v_dot2c_f32_bf16 v18, v193, v193
v_dot2c_f32_bf16 v17, s73, v194
v_dot2c_f32_bf16 v18, v194, v194
v_dot2c_f32_bf16 v17, s73, v195
v_dot2c_f32_bf16 v18, v195, v195
v_dot2c_f32_bf16 v17, s73, v196
v_dot2c_f32_bf16 v18, v196, v196
v_dot2c_f32_bf16 v17, s73, v197
v_dot2c_f32_bf16 v18, v197, v197
v_dot2c_f32_bf16 v17, s73, v198
v_dot2c_f32_bf16 v18, v198, v198
v_dot2c_f32_bf16 v17, s73, v199
v_dot2c_f32_bf16 v18, v199, v199
v_dot2c_f32_bf16 v17, s73, v200
v_dot2c_f32_bf16 v18, v200, v200
v_dot2c_f32_bf16 v17, s73, v201
v_dot2c_f32_bf16 v18, v201, v201
v_dot2c_f32_bf16 v17, s73, v202
v_dot2c_f32_bf16 v18, v202, v202
v_dot2c_f32_bf16 v17, s73, v203
v_dot2c_f32_bf16 v18, v203, v203
v_dot2c_f32_bf16 v17, s73, v204
v_dot2c_f32_bf16 v18, v204, v204
v_dot2c_f32_bf16 v17, s73, v205
v_dot2c_f32_bf16 v18, v205, v205
v_dot2c_f32_bf16 v17, s73, v206
v_dot2c_f32_bf16 v18, v206, v206
v_dot2c_f32_bf16 v17, s73, v207
v_dot2c_f32_bf16 v18, v207, v207
s_waitcnt lgkmcnt(0)
v_dot2c_f32_bf16 v17, s73, v208
v_dot2c_f32_bf16 v18, v208, v208
v_dot2c_f32_bf16 v17, s73, v209
v_dot2c_f32_bf16 v18, v209, v209
v_dot2c_f32_bf16 v17, s73, v210
v_dot2c_f32_bf16 v18, v210, v210
v_dot2c_f32_bf16 v17, s73, v211
v_dot2c_f32_bf16 v18, v211, v211
v_dot2c_f32_bf16 v17, s73, v212
v_dot2c_f32_bf16 v18, v212, v212
v_dot2c_f32_bf16 v17, s73, v213
v_dot2c_f32_bf16 v18, v213, v213
v_dot2c_f32_bf16 v17, s73, v214
v_dot2c_f32_bf16 v18, v214, v214
v_dot2c_f32_bf16 v17, s73, v215
v_dot2c_f32_bf16 v18, v215, v215
v_dot2c_f32_bf16 v17, s73, v216
v_dot2c_f32_bf16 v18, v216, v216
v_dot2c_f32_bf16 v17, s73, v217
v_dot2c_f32_bf16 v18, v217, v217
v_dot2c_f32_bf16 v17, s73, v218
v_dot2c_f32_bf16 v18, v218, v218
v_dot2c_f32_bf16 v17, s73, v219
v_dot2c_f32_bf16 v18, v219, v219
v_dot2c_f32_bf16 v17, s73, v220
v_dot2c_f32_bf16 v18, v220, v220
v_dot2c_f32_bf16 v17, s73, v221
v_dot2c_f32_bf16 v18, v221, v221
v_dot2c_f32_bf16 v17, s73, v222
v_dot2c_f32_bf16 v18, v222, v222
v_dot2c_f32_bf16 v17, s73, v223
v_dot2c_f32_bf16 v18, v223, v223
s_and_b32 exec_lo, -1, s3
s_mov_b32 exec_hi, 0
buffer_load_dword v22, v23, s[16:19], 0 offen sc1
s_mov_b64 exec, -1
v_and_b32 v50, 31, v0
s_lshl_b32 s67, s49, 7
s_add_u32 s67, s67, 159744
v_mov_b32 v48, v17
s_nop 1
v_permlane32_swap_b32 v17, v48
v_add_f32 v17, v17, v48
v_mov_b32 v49, v18
s_nop 1
v_permlane32_swap_b32 v18, v49
v_add_f32 v18, v18, v49
v_lshl_add_u32 v50, v50, 2, s67
v_mul_f32 v17, 0x3b124925, v17
v_mul_f32 v18, 0x3b124925, v18
v_fma_f32 v18, -v17, v17, v18
v_max_f32 v18, 0, v18
v_add_f32 v18, 0x3727c5ac, v18
v_rsq_f32 v20, v18
v_lshrrev_b32 v49, 5, v0
v_lshl_add_u32 v49, v49, 4, s67
ds_write_b32 v50, v20 offset:0
ds_read_b128 v[96:99], v49 offset:0
ds_read_b128 v[100:103], v49 offset:32
ds_read_b128 v[104:107], v49 offset:64
ds_read_b128 v[108:111], v49 offset:96
v_cvt_pk_bf16_f32 v44, -v17, -v17
v_mov_b32 v46, 0
v_lshlrev_b32 v48, 16, v44
v_add_f32 v48, v48, v17
v_mov_b32 v47, 0
v_cvt_pk_bf16_f32 v45, -v48, -v48
s_lshl2_add_u32 s67, s49, 162304
v_add_u32 v50, s67, v1
v_add_u32 v51, 162304, v1
s_bitcmp1_b32 s51, 0
s_cselect_b32 s76, 1, 0x10000000
s_mov_b32 s77, s76
s_lshl2_add_u32 s69, s51, s49
s_lshl4_add_u32 s69, s69, s50
s_lshl_b32 s69, s69, 9
s_add_u32 s69, s69, 2097152
s_add_u32 s67, s57, 20480
v_add_u32 v9, s67, v1
s_lshl_b32 s67, s49, 10
s_add_u32 s67, s67, 20480
v_add_u32 v10, s67, v1
v_add_u32 v11, 57344, v10
v_and_b32 v48, 31, v0
v_lshrrev_b32 v49, 5, v0
v_lshlrev_b32 v49, 3, v49
v_lshl_add_u32 v12, v48, 4, v49
s_waitcnt vmcnt(8)
s_barrier
ds_read_b64_tr_b16 v[64:65], v13 offset:8192
ds_read_b64_tr_b16 v[66:67], v13 offset:8448
ds_read_b64_tr_b16 v[80:81], v13 offset:9216
ds_read_b64_tr_b16 v[82:83], v13 offset:9472
ds_read_b64_tr_b16 v[68:69], v14 offset:8192
ds_read_b64_tr_b16 v[70:71], v14 offset:8448
ds_read_b64_tr_b16 v[84:85], v14 offset:9216
ds_read_b64_tr_b16 v[86:87], v14 offset:9472
ds_read_b64_tr_b16 v[72:73], v15 offset:8192
ds_read_b64_tr_b16 v[74:75], v15 offset:8448
ds_read_b64_tr_b16 v[88:89], v15 offset:9216
ds_read_b64_tr_b16 v[90:91], v15 offset:9472
ds_read_b64_tr_b16 v[76:77], v16 offset:8192
ds_read_b64_tr_b16 v[78:79], v16 offset:8448
ds_read_b64_tr_b16 v[92:93], v16 offset:9216
ds_read_b64_tr_b16 v[94:95], v16 offset:9472
s_add_u32 s67, s49, 0
s_and_b32 s67, s67, 3
s_lshl_b32 s67, s67, 10
v_subrev_u32 v13, s67, v13
s_add_u32 s67, s49, 1
s_and_b32 s67, s67, 3
s_lshl_b32 s67, s67, 10
v_subrev_u32 v14, s67, v14
s_add_u32 s67, s49, 2
s_and_b32 s67, s67, 3
s_lshl_b32 s67, s67, 10
v_subrev_u32 v15, s67, v15
s_add_u32 s67, s49, 3
s_and_b32 s67, s67, 3
s_lshl_b32 s67, s67, 10
v_subrev_u32 v16, s67, v16
s_and_b32 s72, s51, 7
s_andn2_b32 s6, 1, s48
s_add_u32 s72, s72, s6
s_bfe_u32 s72, s72, 0x20001
s_and_b32 s6, s50, 3
s_lshl2_add_u32 s72, s6, s72
s_lshl2_add_u32 s72, s72, s49
v_lshlrev_b32 v34, 7, v0
v_mul_u32_u24 v35, 37, v0
v_lshrrev_b32 v35, 8, v35
v_mul_u32_u24 v35, 2688, v35
v_add_u32 v35, v35, v34
s_mul_i32 s6, s72, 6272
s_mul_i32 s72, s72, 25088
s_and_b32 s7, s48, 1
s_mul_i32 s7, s7, 896
s_add_u32 s7, s7, s72
s_bitcmp1_b32 0x552b, s51
s_cselect_b32 exec_lo, -1, 0
s_cselect_b32 exec_hi, 0x1ffff, 0
buffer_load_dword v36, v34, s[20:23], s6 offen
buffer_load_dword v36, v35, s[24:27], s7 offen offset:1792
s_mov_b64 exec, -1
s_waitcnt lgkmcnt(12)
v_dot2c_f32_bf16 v19, s73, v64
v_dot2c_f32_bf16 v21, s73, v80
v_dot2c_f32_bf16 v19, s73, v65
v_dot2c_f32_bf16 v21, s73, v81
v_dot2c_f32_bf16 v19, s73, v66
v_dot2c_f32_bf16 v21, s73, v82
v_dot2c_f32_bf16 v19, s73, v67
v_dot2c_f32_bf16 v21, s73, v83
s_nop 2
v_permlane32_swap_b32 v19, v21
v_add_f32 v19, v19, v21
ds_write_b32 v50, v19 offset:0
v_mfma_f32_32x32x16_bf16 v[224:239], v[208:211], v[64:67], v[224:239]
v_mfma_f32_32x32x16_bf16 v[240:255], v[208:211], v[80:83], v[240:255]
s_waitcnt lgkmcnt(0)
s_barrier
ds_read_b128 v[60:63], v51 offset:0
v_mfma_f32_32x32x16_bf16 v[224:239], v[212:215], v[68:71], v[224:239]
v_mfma_f32_32x32x16_bf16 v[240:255], v[212:215], v[84:87], v[240:255]
v_mfma_f32_32x32x16_bf16 v[224:239], v[216:219], v[72:75], v[224:239]
s_waitcnt lgkmcnt(0)
v_add_f32 v60, v60, v61
v_add_f32 v62, v62, v63
v_add_f32 v60, v60, v62
v_mfma_f32_32x32x16_bf16 v[240:255], v[216:219], v[88:91], v[240:255]
v_cvt_pk_bf16_f32 v50, v60, v60
v_lshlrev_b32 v50, 16, v50
v_sub_f32 v51, v60, v50
v_cvt_pk_bf16_f32 v50, v50, v51
v_mfma_f32_32x32x16_bf16 v[224:239], v[220:223], v[76:79], v[224:239]
v_cndmask_b32_e64 v52, v50, 0, s[74:75]
v_mov_b32 v54, 0
v_mov_b32 v53, v52
v_mov_b32 v55, 0
v_mfma_f32_32x32x16_bf16 v[240:255], v[220:223], v[92:95], v[240:255]
v_cndmask_b32_e64 v56, 0, v50, s[74:75]
v_mov_b32 v58, 0
v_mov_b32 v57, v56
v_mov_b32 v59, 0
v_mfma_f32_32x32x16_bf16 v[224:239], v[44:47], v[52:55], v[224:239]
s_nop 0
v_mfma_f32_32x32x16_bf16 v[240:255], v[44:47], v[56:59], v[240:255]
s_nop 9
v_pk_fma_f32 v[224:225], v[224:225], v[96:97], s[82:83]
v_pk_fma_f32 v[226:227], v[226:227], v[98:99], s[82:83]
v_pk_fma_f32 v[228:229], v[228:229], v[100:101], s[82:83]
v_pk_fma_f32 v[230:231], v[230:231], v[102:103], s[82:83]
v_pk_mul_f32 v[48:49], v[224:225], s[80:81]
v_pk_mul_f32 v[50:51], v[226:227], s[80:81]
v_pk_mul_f32 v[52:53], v[228:229], s[80:81]
v_pk_mul_f32 v[54:55], v[230:231], s[80:81]
v_exp_f32_e64 v48, v48 clamp
v_max_f32 v224, s82, v224
v_exp_f32_e64 v49, v49 clamp
v_max_f32 v225, s82, v225
v_exp_f32_e64 v50, v50 clamp
v_max_f32 v226, s82, v226
v_exp_f32_e64 v51, v51 clamp
v_max_f32 v227, s82, v227
v_exp_f32_e64 v52, v52 clamp
v_max_f32 v228, s82, v228
v_exp_f32_e64 v53, v53 clamp
v_max_f32 v229, s82, v229
v_exp_f32_e64 v54, v54 clamp
v_max_f32 v230, s82, v230
v_exp_f32_e64 v55, v55 clamp
v_max_f32 v231, s82, v231
v_pk_add_f32 v[48:49], v[48:49], v[224:225]
v_pk_add_f32 v[50:51], v[50:51], v[226:227]
v_pk_add_f32 v[52:53], v[52:53], v[228:229]
v_pk_add_f32 v[54:55], v[54:55], v[230:231]
v_cvt_pk_bf16_f32 v64, v48, v49
v_cvt_pk_bf16_f32 v65, v50, v51
v_cvt_pk_bf16_f32 v66, v52, v53
v_cvt_pk_bf16_f32 v67, v54, v55
v_pk_fma_f32 v[232:233], v[232:233], v[104:105], s[82:83]
v_pk_fma_f32 v[234:235], v[234:235], v[106:107], s[82:83]
v_pk_fma_f32 v[236:237], v[236:237], v[108:109], s[82:83]
v_pk_fma_f32 v[238:239], v[238:239], v[110:111], s[82:83]
v_pk_mul_f32 v[48:49], v[232:233], s[80:81]
v_pk_mul_f32 v[50:51], v[234:235], s[80:81]
v_pk_mul_f32 v[52:53], v[236:237], s[80:81]
v_pk_mul_f32 v[54:55], v[238:239], s[80:81]
v_exp_f32_e64 v48, v48 clamp
v_max_f32 v232, s82, v232
v_exp_f32_e64 v49, v49 clamp
v_max_f32 v233, s82, v233
v_exp_f32_e64 v50, v50 clamp
v_max_f32 v234, s82, v234
v_exp_f32_e64 v51, v51 clamp
v_max_f32 v235, s82, v235
v_exp_f32_e64 v52, v52 clamp
v_max_f32 v236, s82, v236
v_exp_f32_e64 v53, v53 clamp
v_max_f32 v237, s82, v237
v_exp_f32_e64 v54, v54 clamp
v_max_f32 v238, s82, v238
v_exp_f32_e64 v55, v55 clamp
v_max_f32 v239, s82, v239
v_pk_add_f32 v[48:49], v[48:49], v[232:233]
v_pk_add_f32 v[50:51], v[50:51], v[234:235]
v_pk_add_f32 v[52:53], v[52:53], v[236:237]
v_pk_add_f32 v[54:55], v[54:55], v[238:239]
v_cvt_pk_bf16_f32 v68, v48, v49
v_cvt_pk_bf16_f32 v69, v50, v51
v_cvt_pk_bf16_f32 v70, v52, v53
v_cvt_pk_bf16_f32 v71, v54, v55
v_pk_mul_f32 v[240:241], v[240:241], v[96:97]
v_pk_mul_f32 v[242:243], v[242:243], v[98:99]
v_pk_mul_f32 v[244:245], v[244:245], v[100:101]
v_pk_mul_f32 v[246:247], v[246:247], v[102:103]
v_pk_mul_f32 v[248:249], v[248:249], v[104:105]
v_pk_mul_f32 v[250:251], v[250:251], v[106:107]
v_pk_mul_f32 v[252:253], v[252:253], v[108:109]
v_pk_mul_f32 v[254:255], v[254:255], v[110:111]
v_cvt_pk_bf16_f32 v72, v240, v241
v_cvt_pk_bf16_f32 v73, v242, v243
v_cvt_pk_bf16_f32 v74, v244, v245
v_cvt_pk_bf16_f32 v75, v246, v247
v_cvt_pk_bf16_f32 v76, v248, v249
v_cvt_pk_bf16_f32 v77, v250, v251
v_cvt_pk_bf16_f32 v78, v252, v253
v_cvt_pk_bf16_f32 v79, v254, v255
s_mov_b64 exec, s[76:77]
v_mov_b32 v72, s73
v_mov_b32 v73, s73
v_mov_b32 v74, s73
v_mov_b32 v75, s73
v_mov_b32 v76, s73
v_mov_b32 v77, s73
v_mov_b32 v78, s73
v_mov_b32 v79, s73
s_mov_b64 exec, -1
v_mfma_f32_32x32x16_bf16 v[224:239], v[64:67], v[72:75], 0
v_mfma_f32_32x32x16_bf16 v[224:239], v[68:71], v[76:79], v[224:239]
s_nop 11
ds_write_b128 v9, v[224:227] offset:0
ds_write_b128 v9, v[228:231] offset:1024
ds_write_b128 v9, v[232:235] offset:2048
ds_write_b128 v9, v[236:239] offset:3072
s_waitcnt vmcnt(3) lgkmcnt(0)
s_barrier
ds_read_b128 v[240:243], v10 offset:0
ds_read_b128 v[244:247], v10 offset:28672
ds_read_b128 v[248:251], v11 offset:0
ds_read_b128 v[252:255], v11 offset:28672
s_waitcnt lgkmcnt(2)
v_pk_add_f32 v[240:241], v[240:241], v[244:245]
v_pk_add_f32 v[242:243], v[242:243], v[246:247]
s_waitcnt lgkmcnt(0)
v_pk_add_f32 v[248:249], v[248:249], v[252:253]
v_pk_add_f32 v[250:251], v[250:251], v[254:255]
v_pk_add_f32 v[240:241], v[240:241], v[248:249]
v_pk_add_f32 v[242:243], v[242:243], v[250:251]
v_cvt_pk_bf16_f32 v240, v240, v241
v_cvt_pk_bf16_f32 v241, v242, v243
buffer_store_dwordx2 v[240:241], v12, s[16:19], s69 offen sc1
ds_read_b64_tr_b16 v[64:65], v13 offset:16384
ds_read_b64_tr_b16 v[66:67], v13 offset:16640
ds_read_b64_tr_b16 v[68:69], v13 offset:20480
ds_read_b64_tr_b16 v[70:71], v13 offset:20736
ds_read_b64_tr_b16 v[72:73], v13 offset:24576
ds_read_b64_tr_b16 v[74:75], v13 offset:24832
ds_read_b64_tr_b16 v[76:77], v13 offset:28672
ds_read_b64_tr_b16 v[78:79], v13 offset:28928
ds_read_b64_tr_b16 v[80:81], v13 offset:32768
ds_read_b64_tr_b16 v[82:83], v13 offset:33024
ds_read_b64_tr_b16 v[84:85], v13 offset:36864
ds_read_b64_tr_b16 v[86:87], v13 offset:37120
ds_read_b64_tr_b16 v[88:89], v13 offset:40960
ds_read_b64_tr_b16 v[90:91], v13 offset:41216
v_mov_b32 v19, 0
s_lshl2_add_u32 s67, s49, 162304
v_add_u32 v50, s67, v1
s_waitcnt lgkmcnt(0)
v_dot2c_f32_bf16 v19, s73, v64
v_dot2c_f32_bf16 v19, s73, v65
v_dot2c_f32_bf16 v19, s73, v66
v_dot2c_f32_bf16 v19, s73, v67
v_dot2c_f32_bf16 v19, s73, v68
v_dot2c_f32_bf16 v19, s73, v69
v_dot2c_f32_bf16 v19, s73, v70
v_dot2c_f32_bf16 v19, s73, v71
v_dot2c_f32_bf16 v19, s73, v72
v_dot2c_f32_bf16 v19, s73, v73
v_dot2c_f32_bf16 v19, s73, v74
v_dot2c_f32_bf16 v19, s73, v75
v_dot2c_f32_bf16 v19, s73, v76
v_dot2c_f32_bf16 v19, s73, v77
v_dot2c_f32_bf16 v19, s73, v78
v_dot2c_f32_bf16 v19, s73, v79
v_dot2c_f32_bf16 v19, s73, v80
v_dot2c_f32_bf16 v19, s73, v81
v_dot2c_f32_bf16 v19, s73, v82
v_dot2c_f32_bf16 v19, s73, v83
v_dot2c_f32_bf16 v19, s73, v84
v_dot2c_f32_bf16 v19, s73, v85
v_dot2c_f32_bf16 v19, s73, v86
v_dot2c_f32_bf16 v19, s73, v87
v_dot2c_f32_bf16 v19, s73, v88
v_dot2c_f32_bf16 v19, s73, v89
v_dot2c_f32_bf16 v19, s73, v90
v_dot2c_f32_bf16 v19, s73, v91
ds_read_b64_tr_b16 v[224:225], v14 offset:16384
ds_read_b64_tr_b16 v[226:227], v14 offset:16640
ds_read_b64_tr_b16 v[228:229], v15 offset:16384
ds_read_b64_tr_b16 v[230:231], v15 offset:16640
ds_read_b64_tr_b16 v[232:233], v16 offset:16384
ds_read_b64_tr_b16 v[234:235], v16 offset:16640
v_mov_b32 v48, v19
s_nop 1
v_permlane32_swap_b32 v19, v48
v_add_f32 v19, v19, v48
ds_write_b32 v50, v19 offset:0
v_add_u32 v50, 162304, v1
v_mfma_f32_32x32x16_bf16 v[96:111], v[64:67], v[112:115], 0
v_mfma_f32_32x32x16_bf16 v[96:111], v[68:71], v[128:131], v[96:111]
v_mfma_f32_32x32x16_bf16 v[96:111], v[72:75], v[144:147], v[96:111]
v_mfma_f32_32x32x16_bf16 v[96:111], v[76:79], v[160:163], v[96:111]
v_mfma_f32_32x32x16_bf16 v[96:111], v[80:83], v[176:179], v[96:111]
v_mfma_f32_32x32x16_bf16 v[96:111], v[84:87], v[192:195], v[96:111]
v_mfma_f32_32x32x16_bf16 v[96:111], v[88:91], v[208:211], v[96:111]
s_waitcnt lgkmcnt(5)
v_mfma_f32_32x32x16_bf16 v[96:111], v[224:227], v[116:119], v[96:111]
ds_read_b64_tr_b16 v[236:237], v14 offset:20480
ds_read_b64_tr_b16 v[238:239], v14 offset:20736
s_waitcnt vmcnt(0) lgkmcnt(2)
s_barrier
v_and_b32 v48, 1, v22
v_lshl_add_u32 v48, v48, 7, v23
s_and_b32 exec_lo, 0xffff, s3
s_mov_b32 exec_hi, 0
buffer_atomic_add v3, v48, s[16:19], 0 offen offset:128
s_mov_b64 exec, -1
ds_read_b128 v[60:63], v50 offset:0
v_lshrrev_b32 v58, 3, v0
v_xor_b32 v59, v58, v0
v_mul_u32_u24 v58, 896, v58
v_and_b32 v59, 7, v59
v_lshl_add_u32 v58, v59, 4, v58
s_mul_i32 s6, s54, 14336
s_lshl_b32 s7, s65, 7
s_add_u32 s6, s6, s7
s_lshl_b32 s7, s49, 11
s_add_u32 m0, s7, 57344
s_nop 0
buffer_load_dwordx4 v58, s[8:11], s6 offen lds
s_add_u32 s6, s6, 6144
buffer_load_dwordx4 v58, s[8:11], s6 offen offset:1024 lds
v_mfma_f32_32x32x16_bf16 v[96:111], v[228:231], v[120:123], v[96:111]
ds_read_b64_tr_b16 v[224:225], v15 offset:20480
ds_read_b64_tr_b16 v[226:227], v15 offset:20736
s_lshl2_add_u32 s67, s51, s49
s_lshl_b32 s67, s67, 13
v_mfma_f32_32x32x16_bf16 v[96:111], v[232:235], v[124:127], v[96:111]
ds_read_b64_tr_b16 v[228:229], v16 offset:20480
ds_read_b64_tr_b16 v[230:231], v16 offset:20736
s_add_u32 s67, s67, 2097152
s_mul_i32 s6, s49, 14
s_lshl_b32 s7, s6, 10
s_add_u32 m0, s7, 0
s_mul_i32 s7, s84, 7
s_add_u32 s6, s6, s7
s_add_u32 s6, s6, 0
s_mul_i32 s6, s6, 7168
s_cmp_ge_u32 s6, 401408
s_cselect_b32 s7, 401408, 0
s_sub_u32 s6, s6, s7
buffer_load_dwordx4 v32, s[20:23], s6 offen lds
s_waitcnt lgkmcnt(5)
v_mfma_f32_32x32x16_bf16 v[96:111], v[236:239], v[132:135], v[96:111]
ds_read_b64_tr_b16 v[232:233], v14 offset:24576
ds_read_b64_tr_b16 v[234:235], v14 offset:24832
s_and_b32 s68, s50, 3
s_lshl_b32 s68, s68, 11
s_waitcnt lgkmcnt(4)
v_mfma_f32_32x32x16_bf16 v[96:111], v[224:227], v[136:139], v[96:111]
ds_read_b64_tr_b16 v[236:237], v15 offset:24576
ds_read_b64_tr_b16 v[238:239], v15 offset:24832
s_add_u32 m0, m0, 1024
s_add_u32 s6, s6, 7168
s_cmp_ge_u32 s6, 401408
s_cselect_b32 s7, 401408, 0
s_sub_u32 s6, s6, s7
buffer_load_dwordx4 v32, s[20:23], s6 offen lds
s_add_u32 s89, s67, s68
s_add_u32 s68, s68, 1024
s_waitcnt lgkmcnt(4)
v_mfma_f32_32x32x16_bf16 v[96:111], v[228:231], v[140:143], v[96:111]
ds_read_b64_tr_b16 v[224:225], v16 offset:24576
ds_read_b64_tr_b16 v[226:227], v16 offset:24832
s_add_u32 m0, m0, 1024
s_add_u32 s6, s6, 7168
s_cmp_ge_u32 s6, 401408
s_cselect_b32 s7, 401408, 0
s_sub_u32 s6, s6, s7
buffer_load_dwordx4 v32, s[20:23], s6 offen lds
s_and_b32 s68, s68, 0x1fff
s_waitcnt lgkmcnt(4)
v_mfma_f32_32x32x16_bf16 v[96:111], v[232:235], v[148:151], v[96:111]
ds_read_b64_tr_b16 v[228:229], v14 offset:28672
ds_read_b64_tr_b16 v[230:231], v14 offset:28928
s_add_u32 s90, s67, s68
s_add_u32 m0, m0, 1024
s_add_u32 s6, s6, 7168
s_cmp_ge_u32 s6, 401408
s_cselect_b32 s7, 401408, 0
s_sub_u32 s6, s6, s7
buffer_load_dwordx4 v32, s[20:23], s6 offen lds
s_waitcnt lgkmcnt(4)
v_mfma_f32_32x32x16_bf16 v[96:111], v[236:239], v[152:155], v[96:111]
ds_read_b64_tr_b16 v[232:233], v15 offset:28672
ds_read_b64_tr_b16 v[234:235], v15 offset:28928
s_add_u32 s68, s68, 1024
s_and_b32 s68, s68, 0x1fff
s_add_u32 m0, m0, 1024
s_add_u32 s6, s6, 7168
s_cmp_ge_u32 s6, 401408
s_cselect_b32 s7, 401408, 0
s_sub_u32 s6, s6, s7
buffer_load_dwordx4 v32, s[20:23], s6 offen lds
s_waitcnt lgkmcnt(4)
v_mfma_f32_32x32x16_bf16 v[96:111], v[224:227], v[156:159], v[96:111]
ds_read_b64_tr_b16 v[236:237], v16 offset:28672
ds_read_b64_tr_b16 v[238:239], v16 offset:28928
s_add_u32 s91, s67, s68
s_add_u32 s68, s68, 1024
s_waitcnt lgkmcnt(4)
v_mfma_f32_32x32x16_bf16 v[96:111], v[228:231], v[164:167], v[96:111]
ds_read_b64_tr_b16 v[224:225], v14 offset:32768
ds_read_b64_tr_b16 v[226:227], v14 offset:33024
s_add_u32 m0, m0, 1024
s_add_u32 s6, s6, 7168
s_cmp_ge_u32 s6, 401408
s_cselect_b32 s7, 401408, 0
s_sub_u32 s6, s6, s7
buffer_load_dwordx4 v32, s[20:23], s6 offen lds
s_and_b32 s68, s68, 0x1fff
s_waitcnt lgkmcnt(4)
v_mfma_f32_32x32x16_bf16 v[96:111], v[232:235], v[168:171], v[96:111]
ds_read_b64_tr_b16 v[228:229], v15 offset:32768
ds_read_b64_tr_b16 v[230:231], v15 offset:33024
s_add_u32 s92, s67, s68
s_add_u32 m0, m0, 1024
s_add_u32 s6, s6, 7168
s_cmp_ge_u32 s6, 401408
s_cselect_b32 s7, 401408, 0
s_sub_u32 s6, s6, s7
buffer_load_dwordx4 v32, s[20:23], s6 offen lds
s_add_u32 s68, s68, 1024
s_waitcnt lgkmcnt(4)
v_mfma_f32_32x32x16_bf16 v[96:111], v[236:239], v[172:175], v[96:111]
ds_read_b64_tr_b16 v[232:233], v16 offset:32768
ds_read_b64_tr_b16 v[234:235], v16 offset:33024
s_and_b32 s68, s68, 0x1fff
s_add_u32 m0, m0, 1024
s_add_u32 s6, s6, 7168
s_cmp_ge_u32 s6, 401408
s_cselect_b32 s7, 401408, 0
s_sub_u32 s6, s6, s7
buffer_load_dwordx4 v32, s[20:23], s6 offen lds
s_waitcnt lgkmcnt(4)
v_mfma_f32_32x32x16_bf16 v[96:111], v[224:227], v[180:183], v[96:111]
ds_read_b64_tr_b16 v[236:237], v14 offset:36864
ds_read_b64_tr_b16 v[238:239], v14 offset:37120
s_add_u32 s93, s67, s68
s_add_u32 s68, s68, 1024
s_waitcnt lgkmcnt(4)
v_mfma_f32_32x32x16_bf16 v[96:111], v[228:231], v[184:187], v[96:111]
ds_read_b64_tr_b16 v[224:225], v15 offset:36864
ds_read_b64_tr_b16 v[226:227], v15 offset:37120
s_add_u32 m0, m0, 1024
s_add_u32 s6, s6, 7168
s_cmp_ge_u32 s6, 401408
s_cselect_b32 s7, 401408, 0
s_sub_u32 s6, s6, s7
buffer_load_dwordx4 v32, s[20:23], s6 offen lds
s_and_b32 s68, s68, 0x1fff
s_waitcnt lgkmcnt(4)
v_mfma_f32_32x32x16_bf16 v[96:111], v[232:235], v[188:191], v[96:111]
ds_read_b64_tr_b16 v[228:229], v16 offset:36864
ds_read_b64_tr_b16 v[230:231], v16 offset:37120
s_add_u32 s94, s67, s68
s_add_u32 m0, m0, 1024
s_add_u32 s6, s6, 7168
s_cmp_ge_u32 s6, 401408
s_cselect_b32 s7, 401408, 0
s_sub_u32 s6, s6, s7
buffer_load_dwordx4 v32, s[20:23], s6 offen lds
s_add_u32 s68, s68, 1024
s_waitcnt lgkmcnt(4)
v_mfma_f32_32x32x16_bf16 v[96:111], v[236:239], v[196:199], v[96:111]
ds_read_b64_tr_b16 v[232:233], v14 offset:40960
ds_read_b64_tr_b16 v[234:235], v14 offset:41216
s_and_b32 s68, s68, 0x1fff
s_add_u32 m0, m0, 1024
s_add_u32 s6, s6, 7168
s_cmp_ge_u32 s6, 401408
s_cselect_b32 s7, 401408, 0
s_sub_u32 s6, s6, s7
buffer_load_dwordx4 v32, s[20:23], s6 offen lds
s_waitcnt lgkmcnt(4)
v_mfma_f32_32x32x16_bf16 v[96:111], v[224:227], v[200:203], v[96:111]
ds_read_b64_tr_b16 v[236:237], v15 offset:40960
ds_read_b64_tr_b16 v[238:239], v15 offset:41216
s_add_u32 s95, s67, s68
s_add_u32 s68, s68, 1024
s_waitcnt lgkmcnt(4)
v_mfma_f32_32x32x16_bf16 v[96:111], v[228:231], v[204:207], v[96:111]
ds_read_b64_tr_b16 v[224:225], v16 offset:40960
ds_read_b64_tr_b16 v[226:227], v16 offset:41216
s_add_u32 m0, m0, 1024
s_add_u32 s6, s6, 7168
s_cmp_ge_u32 s6, 401408
s_cselect_b32 s7, 401408, 0
s_sub_u32 s6, s6, s7
buffer_load_dwordx4 v32, s[20:23], s6 offen lds
s_and_b32 s68, s68, 0x1fff
s_add_u32 s96, s67, s68
s_waitcnt lgkmcnt(4)
v_mfma_f32_32x32x16_bf16 v[96:111], v[232:235], v[212:215], v[96:111]
s_add_u32 m0, m0, 1024
s_add_u32 s6, s6, 7168
s_cmp_ge_u32 s6, 401408
s_cselect_b32 s7, 401408, 0
s_sub_u32 s6, s6, s7
buffer_load_dwordx4 v32, s[20:23], s6 offen lds
s_add_u32 s68, s68, 1024
s_waitcnt lgkmcnt(2)
v_mfma_f32_32x32x16_bf16 v[96:111], v[236:239], v[216:219], v[96:111]
s_and_b32 s68, s68, 0x1fff
s_add_u32 m0, m0, 1024
s_add_u32 s6, s6, 7168
s_cmp_ge_u32 s6, 401408
s_cselect_b32 s7, 401408, 0
s_sub_u32 s6, s6, s7
buffer_load_dwordx4 v32, s[20:23], s6 offen lds
s_waitcnt lgkmcnt(0)
v_mfma_f32_32x32x16_bf16 v[96:111], v[224:227], v[220:223], v[96:111]
s_mov_b32 s97, 0x3f80
s_mov_b32 s98, 0x3f800000
v_add_f32 v60, v60, v61
v_add_f32 v62, v62, v63
v_add_f32 v60, v60, v62
v_cvt_pk_bf16_f32 v50, v60, v60
v_lshlrev_b32 v50, 16, v50
v_sub_f32 v51, v60, v50
v_cvt_pk_bf16_f32 v50, v50, v51
v_cndmask_b32_e64 v52, v50, 0, s[74:75]
v_mov_b32 v54, 0
v_mov_b32 v53, v52
v_mov_b32 v55, 0
s_nop 1
v_mfma_f32_32x32x16_bf16 v[96:111], v[52:55], v[44:47], v[96:111]
v_mov_b32 v144, 0
v_mov_b32 v145, 0
v_mov_b32 v146, 0
v_mov_b32 v147, 0
v_mov_b32 v148, 0
v_mov_b32 v149, 0
v_mov_b32 v150, 0
v_mov_b32 v151, 0
s_and_b32 exec_lo, 1, s3
s_mov_b32 exec_hi, 0
s_mov_b32 s66, 32768
.Lk_poll_2:
buffer_load_dword v2, off, s[16:19], s61 sc1
s_waitcnt vmcnt(0)
v_cmp_ne_u32_e32 vcc, s60, v2
s_nop 4
s_cbranch_vccz .Lk_done_3
s_sub_u32 s66, s66, 1
s_cbranch_scc0 .Lk_poll_2
.Lk_done_3:
s_mov_b64 exec, -1
s_barrier
buffer_load_dwordx4 v[112:115], v1, s[16:19], s89 offen sc1
buffer_load_dwordx4 v[116:119], v1, s[16:19], s90 offen sc1
buffer_load_dwordx4 v[120:123], v1, s[16:19], s91 offen sc1
buffer_load_dwordx4 v[124:127], v1, s[16:19], s92 offen sc1
buffer_load_dwordx4 v[128:131], v1, s[16:19], s93 offen sc1
buffer_load_dwordx4 v[132:135], v1, s[16:19], s94 offen sc1
buffer_load_dwordx4 v[136:139], v1, s[16:19], s95 offen sc1
buffer_load_dwordx4 v[140:143], v1, s[16:19], s96 offen sc1
s_and_b32 exec_lo, 0xffff, s3
s_mov_b32 exec_hi, 0
buffer_load_dword v21, v23, s[16:19], 0 offen sc1
s_mov_b64 exec, -1
v_and_b32 v58, 15, v0
v_lshrrev_b32 v59, 5, v0
v_xor_b32 v59, v59, v0
v_and_b32 v59, 7, v59
s_lshl_b32 s69, s49, 11
s_add_u32 s69, s69, 57344
v_lshl_add_u32 v58, v58, 7, s69
v_lshl_add_u32 v58, v59, 4, v58
v_bfe_u32 v59, v0, 4, 1
v_lshl_add_u32 v58, v59, 3, v58
ds_read_b64 v[34:35], v58 offset:0
v_xor_b32 v59, 32, v58
ds_read_b64 v[36:37], v59 offset:0
v_xor_b32 v59, 64, v58
ds_read_b64 v[38:39], v59 offset:0
v_xor_b32 v59, 96, v58
ds_read_b64 v[40:41], v59 offset:0
v_pk_fma_f32 v[96:97], v[96:97], v[20:21], s[82:83] op_sel_hi:[1,0,1]
v_pk_fma_f32 v[98:99], v[98:99], v[20:21], s[82:83] op_sel_hi:[1,0,1]
v_pk_fma_f32 v[100:101], v[100:101], v[20:21], s[82:83] op_sel_hi:[1,0,1]
v_pk_fma_f32 v[102:103], v[102:103], v[20:21], s[82:83] op_sel_hi:[1,0,1]
v_pk_mul_f32 v[48:49], v[96:97], s[80:81]
v_pk_mul_f32 v[50:51], v[98:99], s[80:81]
v_pk_mul_f32 v[52:53], v[100:101], s[80:81]
v_pk_mul_f32 v[54:55], v[102:103], s[80:81]
v_exp_f32_e64 v48, v48 clamp
v_max_f32 v96, s82, v96
v_exp_f32_e64 v49, v49 clamp
v_max_f32 v97, s82, v97
v_exp_f32_e64 v50, v50 clamp
v_max_f32 v98, s82, v98
v_exp_f32_e64 v51, v51 clamp
v_max_f32 v99, s82, v99
v_exp_f32_e64 v52, v52 clamp
v_max_f32 v100, s82, v100
v_exp_f32_e64 v53, v53 clamp
v_max_f32 v101, s82, v101
v_exp_f32_e64 v54, v54 clamp
v_max_f32 v102, s82, v102
v_exp_f32_e64 v55, v55 clamp
v_max_f32 v103, s82, v103
v_pk_add_f32 v[48:49], v[48:49], v[96:97]
v_pk_add_f32 v[50:51], v[50:51], v[98:99]
v_pk_add_f32 v[52:53], v[52:53], v[100:101]
v_pk_add_f32 v[54:55], v[54:55], v[102:103]
v_cvt_pk_bf16_f32 v224, v48, v49
v_cvt_pk_bf16_f32 v225, v50, v51
v_cvt_pk_bf16_f32 v226, v52, v53
v_cvt_pk_bf16_f32 v227, v54, v55
v_pk_fma_f32 v[104:105], v[104:105], v[20:21], s[82:83] op_sel_hi:[1,0,1]
v_pk_fma_f32 v[106:107], v[106:107], v[20:21], s[82:83] op_sel_hi:[1,0,1]
v_pk_fma_f32 v[108:109], v[108:109], v[20:21], s[82:83] op_sel_hi:[1,0,1]
v_pk_fma_f32 v[110:111], v[110:111], v[20:21], s[82:83] op_sel_hi:[1,0,1]
v_pk_mul_f32 v[48:49], v[104:105], s[80:81]
v_pk_mul_f32 v[50:51], v[106:107], s[80:81]
v_pk_mul_f32 v[52:53], v[108:109], s[80:81]
v_pk_mul_f32 v[54:55], v[110:111], s[80:81]
v_exp_f32_e64 v48, v48 clamp
v_max_f32 v104, s82, v104
v_exp_f32_e64 v49, v49 clamp
v_max_f32 v105, s82, v105
v_exp_f32_e64 v50, v50 clamp
v_max_f32 v106, s82, v106
v_exp_f32_e64 v51, v51 clamp
v_max_f32 v107, s82, v107
v_exp_f32_e64 v52, v52 clamp
v_max_f32 v108, s82, v108
v_exp_f32_e64 v53, v53 clamp
v_max_f32 v109, s82, v109
v_exp_f32_e64 v54, v54 clamp
v_max_f32 v110, s82, v110
v_exp_f32_e64 v55, v55 clamp
v_max_f32 v111, s82, v111
v_pk_add_f32 v[48:49], v[48:49], v[104:105]
v_pk_add_f32 v[50:51], v[50:51], v[106:107]
v_pk_add_f32 v[52:53], v[52:53], v[108:109]
v_pk_add_f32 v[54:55], v[54:55], v[110:111]
v_cvt_pk_bf16_f32 v228, v48, v49
v_cvt_pk_bf16_f32 v229, v50, v51
v_cvt_pk_bf16_f32 v230, v52, v53
v_cvt_pk_bf16_f32 v231, v54, v55
s_and_b32 s67, s51, 1
s_mul_i32 s68, s67, 3
s_add_u32 s68, s68, s49
s_lshl_b32 s67, s67, 5
s_lshr_b64 s[78:79], s[74:75], s67
s_cmp_eq_u32 s68, 3
s_cselect_b64 s[76:77], s[78:79], 0
s_lshr_b32 s67, s49, 1
s_lshl_b32 s67, s67, 10
s_and_b32 s68, s49, 1
s_lshl3_add_u32 s67, s68, s67
s_add_u32 s67, s67, 160256
v_add_u32 v48, s67, v1
v_add_u32 v49, 160256, v1
v_bfe_u32 v60, v0, 4, 1
v_lshlrev_b32 v60, 14, v60
v_lshrrev_b32 v61, 5, v0
v_lshl_add_u32 v60, v61, 8, v60
v_and_b32 v61, 15, v0
v_lshl_add_u32 v60, v61, 4, v60
s_lshl2_add_u32 s67, s50, s49
s_lshl_b32 s67, s67, 5
s_add_u32 s67, s67, s51
s_lshl_b32 s67, s67, 10
s_add_u32 s67, s67, 4194304
s_waitcnt vmcnt(8)
v_dot2c_f32_bf16 v144, s97, v112
v_dot2c_f32_bf16 v145, s98, v112
v_dot2c_f32_bf16 v146, s97, v113
v_dot2c_f32_bf16 v147, s98, v113
v_dot2c_f32_bf16 v148, s97, v114
v_dot2c_f32_bf16 v149, s98, v114
v_dot2c_f32_bf16 v150, s97, v115
v_dot2c_f32_bf16 v151, s98, v115
s_waitcnt vmcnt(7)
v_dot2c_f32_bf16 v144, s97, v116
v_dot2c_f32_bf16 v145, s98, v116
v_dot2c_f32_bf16 v146, s97, v117
v_dot2c_f32_bf16 v147, s98, v117
v_dot2c_f32_bf16 v148, s97, v118
v_dot2c_f32_bf16 v149, s98, v118
v_dot2c_f32_bf16 v150, s97, v119
v_dot2c_f32_bf16 v151, s98, v119
s_waitcnt vmcnt(6)
v_dot2c_f32_bf16 v144, s97, v120
v_dot2c_f32_bf16 v145, s98, v120
v_dot2c_f32_bf16 v146, s97, v121
v_dot2c_f32_bf16 v147, s98, v121
v_dot2c_f32_bf16 v148, s97, v122
v_dot2c_f32_bf16 v149, s98, v122
v_dot2c_f32_bf16 v150, s97, v123
v_dot2c_f32_bf16 v151, s98, v123
s_waitcnt vmcnt(5)
v_dot2c_f32_bf16 v144, s97, v124
v_dot2c_f32_bf16 v145, s98, v124
v_dot2c_f32_bf16 v146, s97, v125
v_dot2c_f32_bf16 v147, s98, v125
v_dot2c_f32_bf16 v148, s97, v126
v_dot2c_f32_bf16 v149, s98, v126
v_dot2c_f32_bf16 v150, s97, v127
v_dot2c_f32_bf16 v151, s98, v127
s_waitcnt vmcnt(4)
v_dot2c_f32_bf16 v144, s97, v128
v_dot2c_f32_bf16 v145, s98, v128
v_dot2c_f32_bf16 v146, s97, v129
v_dot2c_f32_bf16 v147, s98, v129
v_dot2c_f32_bf16 v148, s97, v130
v_dot2c_f32_bf16 v149, s98, v130
v_dot2c_f32_bf16 v150, s97, v131
v_dot2c_f32_bf16 v151, s98, v131
s_waitcnt vmcnt(3)
v_dot2c_f32_bf16 v144, s97, v132
v_dot2c_f32_bf16 v145, s98, v132
v_dot2c_f32_bf16 v146, s97, v133
v_dot2c_f32_bf16 v147, s98, v133
v_dot2c_f32_bf16 v148, s97, v134
v_dot2c_f32_bf16 v149, s98, v134
v_dot2c_f32_bf16 v150, s97, v135
v_dot2c_f32_bf16 v151, s98, v135
s_waitcnt vmcnt(2)
v_dot2c_f32_bf16 v144, s97, v136
v_dot2c_f32_bf16 v145, s98, v136
v_dot2c_f32_bf16 v146, s97, v137
v_dot2c_f32_bf16 v147, s98, v137
v_dot2c_f32_bf16 v148, s97, v138
v_dot2c_f32_bf16 v149, s98, v138
v_dot2c_f32_bf16 v150, s97, v139
v_dot2c_f32_bf16 v151, s98, v139
s_waitcnt vmcnt(1)
v_dot2c_f32_bf16 v144, s97, v140
v_dot2c_f32_bf16 v145, s98, v140
v_dot2c_f32_bf16 v146, s97, v141
v_dot2c_f32_bf16 v147, s98, v141
v_dot2c_f32_bf16 v148, s97, v142
v_dot2c_f32_bf16 v149, s98, v142
v_dot2c_f32_bf16 v150, s97, v143
v_dot2c_f32_bf16 v151, s98, v143
v_permlane32_swap_b32 v144, v148
v_permlane32_swap_b32 v145, v149
v_permlane32_swap_b32 v146, v150
v_permlane32_swap_b32 v147, v151
v_pk_add_f32 v[144:145], v[144:145], v[148:149]
v_pk_add_f32 v[146:147], v[146:147], v[150:151]
v_cvt_pk_bf16_f32 v50, v144, v145
v_cvt_pk_bf16_f32 v51, v146, v147
v_cndmask_b32_e64 v50, v50, 0, s[76:77]
v_cndmask_b32_e64 v51, v51, 0, s[76:77]
ds_write_b64 v48, v[50:51] offset:0
s_bitcmp1_b32 s51, 0
s_cselect_b64 s[76:77], -1, 0
s_and_b64 s[70:71], s[78:79], s[76:77]
s_andn2_b64 s[78:79], s[78:79], s[76:77]
s_waitcnt lgkmcnt(0)
s_barrier
ds_read_b128 v[64:67], v49 offset:0
ds_read_b128 v[68:71], v49 offset:1024
s_waitcnt lgkmcnt(1)
v_mfma_f32_32x32x16_bf16 v[96:111], v[64:67], v[224:227], 0
s_waitcnt lgkmcnt(0)
v_mfma_f32_32x32x16_bf16 v[96:111], v[68:71], v[228:231], v[96:111]
s_nop 11
v_cndmask_b32_e64 v50, v108, v96, s[76:77]
v_mov_b32 v51, v50
s_nop 1
v_permlane32_swap_b32 v50, v51
v_cndmask_b32_e64 v50, v51, v50, s[76:77]
v_add_f32 v50, s82, v50
v_rcp_f32 v50, v50
s_nop 0
v_pk_mul_f32 v[96:97], v[96:97], v[50:51] op_sel_hi:[1,0]
v_pk_mul_f32 v[98:99], v[98:99], v[50:51] op_sel_hi:[1,0]
v_pk_mul_f32 v[100:101], v[100:101], v[50:51] op_sel_hi:[1,0]
v_pk_mul_f32 v[102:103], v[102:103], v[50:51] op_sel_hi:[1,0]
v_cvt_pk_bf16_f32 v52, v96, v97
v_cvt_pk_bf16_f32 v53, v98, v99
v_cvt_pk_bf16_f32 v54, v100, v101
v_cvt_pk_bf16_f32 v55, v102, v103
v_cndmask_b32_e64 v52, v52, 0, s[70:71]
v_cndmask_b32_e64 v53, v53, 0, s[70:71]
buffer_store_dwordx4 v[52:55], v60, s[16:19], s67 offen sc1
v_pk_mul_f32 v[104:105], v[104:105], v[50:51] op_sel_hi:[1,0]
v_pk_mul_f32 v[106:107], v[106:107], v[50:51] op_sel_hi:[1,0]
v_pk_mul_f32 v[108:109], v[108:109], v[50:51] op_sel_hi:[1,0]
v_pk_mul_f32 v[110:111], v[110:111], v[50:51] op_sel_hi:[1,0]
v_cvt_pk_bf16_f32 v56, v104, v105
v_cvt_pk_bf16_f32 v57, v106, v107
v_cvt_pk_bf16_f32 v58, v108, v109
v_cvt_pk_bf16_f32 v59, v110, v111
v_cndmask_b32_e64 v58, v58, 0, s[78:79]
v_cndmask_b32_e64 v59, v59, 0, s[78:79]
buffer_store_dwordx4 v[56:59], v60, s[16:19], s67 offen offset:512 sc1
s_mov_b32 s51, s64
v_lshrrev_b32 v48, 3, v0
v_lshrrev_b32 v49, 4, v0
v_and_b32 v50, 7, v0
v_xor_b32 v50, v50, v49
v_lshlrev_b32 v48, 12, v48
v_lshl_add_u32 v42, v50, 4, v48
v_xor_b32 v4, 64, v42
v_add_u32 v4, 31744, v4
s_mul_i32 s67, s49, 11264
s_add_u32 s57, s67, 114688
s_mul_i32 s58, s49, 14336
v_and_b32 v48, 15, v0
v_bfe_u32 v50, v0, 1, 3
v_xor_b32 v50, v50, v49
v_lshlrev_b32 v48, 7, v48
v_lshl_add_u32 v5, v50, 4, v48
s_cmp_ge_u32 s51, 14
s_cbranch_scc1 .Lk_idle
v_lshrrev_b32 v48, 5, v0
v_bfe_u32 v49, v0, 4, 1
v_bfe_u32 v50, v0, 2, 2
v_lshl_add_u32 v50, v49, 2, v50
v_lshl_add_u32 v48, v48, 4, v50
v_lshrrev_b32 v50, 1, v50
v_lshlrev_b32 v48, 7, v48
v_lshl_add_u32 v48, v50, 5, v48
v_and_b32 v49, 3, v0
v_lshl_add_u32 v8, v49, 3, v48
v_xor_b32 v9, 32, v8
v_xor_b32 v10, 64, v8
v_xor_b32 v11, 96, v8
v_lshrrev_b32 v48, 4, v0
v_and_b32 v49, 15, v0
v_lshlrev_b32 v49, 4, v49
v_lshl_add_u32 v12, v48, 9, v49
v_lshrrev_b32 v48, 5, v0
v_lshrrev_b32 v49, 2, v49
v_lshl_add_u32 v13, v48, 6, v49
v_mov_b32 v14, 0
v_mov_b32 v15, 0
ds_read_b64_tr_b16 a[0:1], v8 offset:0
ds_read_b64_tr_b16 a[2:3], v8 offset:1024
ds_read_b64_tr_b16 a[4:5], v9 offset:0
ds_read_b64_tr_b16 a[6:7], v9 offset:1024
ds_read_b64_tr_b16 a[8:9], v10 offset:0
ds_read_b64_tr_b16 a[10:11], v10 offset:1024
ds_read_b64_tr_b16 a[12:13], v11 offset:0
ds_read_b64_tr_b16 a[14:15], v11 offset:1024
ds_read_b64_tr_b16 a[16:17], v8 offset:3072
ds_read_b64_tr_b16 a[18:19], v8 offset:4096
ds_read_b64_tr_b16 a[20:21], v9 offset:3072
ds_read_b64_tr_b16 a[22:23], v9 offset:4096
ds_read_b64_tr_b16 a[24:25], v10 offset:3072
ds_read_b64_tr_b16 a[26:27], v10 offset:4096
ds_read_b64_tr_b16 a[28:29], v11 offset:3072
ds_read_b64_tr_b16 a[30:31], v11 offset:4096
ds_read_b64_tr_b16 a[32:33], v8 offset:7168
ds_read_b64_tr_b16 a[34:35], v8 offset:8192
ds_read_b64_tr_b16 a[36:37], v9 offset:7168
ds_read_b64_tr_b16 a[38:39], v9 offset:8192
ds_read_b64_tr_b16 a[40:41], v10 offset:7168
ds_read_b64_tr_b16 a[42:43], v10 offset:8192
ds_read_b64_tr_b16 a[44:45], v11 offset:7168
ds_read_b64_tr_b16 a[46:47], v11 offset:8192
ds_read_b64_tr_b16 a[48:49], v8 offset:10240
ds_read_b64_tr_b16 a[50:51], v8 offset:11264
ds_read_b64_tr_b16 a[52:53], v9 offset:10240
ds_read_b64_tr_b16 a[54:55], v9 offset:11264
ds_read_b64_tr_b16 a[56:57], v10 offset:10240
ds_read_b64_tr_b16 a[58:59], v10 offset:11264
ds_read_b64_tr_b16 a[60:61], v11 offset:10240
ds_read_b64_tr_b16 a[62:63], v11 offset:11264
ds_read_b64_tr_b16 a[64:65], v8 offset:14336
ds_read_b64_tr_b16 a[66:67], v8 offset:15360
ds_read_b64_tr_b16 a[68:69], v9 offset:14336
ds_read_b64_tr_b16 a[70:71], v9 offset:15360
ds_read_b64_tr_b16 a[72:73], v10 offset:14336
ds_read_b64_tr_b16 a[74:75], v10 offset:15360
ds_read_b64_tr_b16 a[76:77], v11 offset:14336
ds_read_b64_tr_b16 a[78:79], v11 offset:15360
ds_read_b64_tr_b16 a[80:81], v8 offset:17408
ds_read_b64_tr_b16 a[82:83], v8 offset:18432
ds_read_b64_tr_b16 a[84:85], v9 offset:17408
ds_read_b64_tr_b16 a[86:87], v9 offset:18432
ds_read_b64_tr_b16 a[88:89], v10 offset:17408
ds_read_b64_tr_b16 a[90:91], v10 offset:18432
ds_read_b64_tr_b16 a[92:93], v11 offset:17408
ds_read_b64_tr_b16 a[94:95], v11 offset:18432
s_waitcnt vmcnt(0)
s_barrier
v_and_b32 v48, 1, v22
v_lshl_add_u32 v48, v48, 7, v23
s_and_b32 exec_lo, 0xffff0000, s3
s_mov_b32 exec_hi, 0
buffer_atomic_add v3, v48, s[16:19], 0 offen offset:384
s_mov_b64 exec, -1
v_xor_b32 v48, v21, v22
v_and_b32 v48, 1, v48
v_cmp_ne_u32_e64 s[76:77], 0, v48
s_nop 0
s_and_b32 s76, s76, 0xffff
s_and_b32 s76, s76, s3
s_cbranch_scc0 .Lk_fresh_4
v_and_b32 v48, 1, v21
v_lshl_add_u32 v48, v48, 7, v23
s_mov_b32 exec_lo, s76
s_mov_b32 exec_hi, 0
buffer_atomic_add v3, v48, s[16:19], 0 offen offset:128
s_waitcnt vmcnt(0)
s_mov_b64 exec, -1
.Lk_fresh_4:
s_mul_i32 s6, s49, 14
s_lshl_b32 s7, s6, 10
s_add_u32 m0, s7, 57344
s_mul_i32 s7, s85, 8
s_add_u32 s6, s6, s7
s_add_u32 s6, s6, 0
s_mul_i32 s6, s6, 28672
s_add_u32 s6, s6, 1792
s_cmp_ge_u32 s6, 1605632
s_cselect_b32 s7, 1605632, 0
s_sub_u32 s6, s6, s7
buffer_load_dwordx4 v33, s[24:27], s6 offen lds
s_lshl_b32 s67, s54, 14
ds_read_b64_tr_b16 a[96:97], v8 offset:21504
ds_read_b64_tr_b16 a[98:99], v8 offset:22528
ds_read_b64_tr_b16 a[100:101], v9 offset:21504
s_add_u32 s67, s67, 4194304
ds_read_b64_tr_b16 a[102:103], v9 offset:22528
ds_read_b64_tr_b16 a[104:105], v10 offset:21504
ds_read_b64_tr_b16 a[106:107], v10 offset:22528
s_add_u32 m0, m0, 1024
s_add_u32 s6, s6, 28672
s_cmp_ge_u32 s6, 1605632
s_cselect_b32 s7, 1605632, 0
s_sub_u32 s6, s6, s7
buffer_load_dwordx4 v33, s[24:27], s6 offen lds
s_lshl_b32 s68, s84, 11
ds_read_b64_tr_b16 a[108:109], v11 offset:21504
ds_read_b64_tr_b16 a[110:111], v11 offset:22528
ds_read_b64_tr_b16 a[112:113], v8 offset:24576
s_add_u32 s89, s67, s68
ds_read_b64_tr_b16 a[114:115], v8 offset:25600
ds_read_b64_tr_b16 a[116:117], v9 offset:24576
ds_read_b64_tr_b16 a[118:119], v9 offset:25600
s_add_u32 m0, m0, 1024
s_add_u32 s6, s6, 28672
s_cmp_ge_u32 s6, 1605632
s_cselect_b32 s7, 1605632, 0
s_sub_u32 s6, s6, s7
buffer_load_dwordx4 v33, s[24:27], s6 offen lds
s_add_u32 s68, s68, 2048
ds_read_b64_tr_b16 a[120:121], v10 offset:24576
ds_read_b64_tr_b16 a[122:123], v10 offset:25600
ds_read_b64_tr_b16 a[124:125], v11 offset:24576
s_and_b32 s68, s68, 0x3fff
ds_read_b64_tr_b16 a[126:127], v11 offset:25600
ds_read_b64_tr_b16 a[128:129], v8 offset:28672
s_add_u32 m0, m0, 1024
s_add_u32 s6, s6, 28672
s_cmp_ge_u32 s6, 1605632
s_cselect_b32 s7, 1605632, 0
s_sub_u32 s6, s6, s7
buffer_load_dwordx4 v33, s[24:27], s6 offen lds
ds_read_b64_tr_b16 a[130:131], v8 offset:29696
s_add_u32 s90, s67, s68
ds_read_b64_tr_b16 a[132:133], v9 offset:28672
ds_read_b64_tr_b16 a[134:135], v9 offset:29696
ds_read_b64_tr_b16 a[136:137], v10 offset:28672
s_add_u32 s68, s68, 2048
ds_read_b64_tr_b16 a[138:139], v10 offset:29696
ds_read_b64_tr_b16 a[140:141], v11 offset:28672
s_add_u32 m0, m0, 1024
s_add_u32 s6, s6, 28672
s_cmp_ge_u32 s6, 1605632
s_cselect_b32 s7, 1605632, 0
s_sub_u32 s6, s6, s7
buffer_load_dwordx4 v33, s[24:27], s6 offen lds
ds_read_b64_tr_b16 a[142:143], v11 offset:29696
s_and_b32 s68, s68, 0x3fff
ds_read_b64_tr_b16 a[144:145], v8 offset:31744
ds_read_b64_tr_b16 a[146:147], v8 offset:32768
ds_read_b64_tr_b16 a[148:149], v9 offset:31744
s_add_u32 s91, s67, s68
ds_read_b64_tr_b16 a[150:151], v9 offset:32768
ds_read_b64_tr_b16 a[152:153], v10 offset:31744
s_add_u32 m0, m0, 1024
s_add_u32 s6, s6, 28672
s_cmp_ge_u32 s6, 1605632
s_cselect_b32 s7, 1605632, 0
s_sub_u32 s6, s6, s7
buffer_load_dwordx4 v33, s[24:27], s6 offen lds
ds_read_b64_tr_b16 a[154:155], v10 offset:32768
s_add_u32 s68, s68, 2048
ds_read_b64_tr_b16 a[156:157], v11 offset:31744
ds_read_b64_tr_b16 a[158:159], v11 offset:32768
ds_read_b64_tr_b16 a[160:161], v8 offset:35840
s_and_b32 s68, s68, 0x3fff
ds_read_b64_tr_b16 a[162:163], v8 offset:36864
s_add_u32 m0, m0, 1024
s_add_u32 s6, s6, 28672
s_cmp_ge_u32 s6, 1605632
s_cselect_b32 s7, 1605632, 0
s_sub_u32 s6, s6, s7
buffer_load_dwordx4 v33, s[24:27], s6 offen lds
ds_read_b64_tr_b16 a[164:165], v9 offset:35840
ds_read_b64_tr_b16 a[166:167], v9 offset:36864
s_add_u32 s92, s67, s68
ds_read_b64_tr_b16 a[168:169], v10 offset:35840
ds_read_b64_tr_b16 a[170:171], v10 offset:36864
ds_read_b64_tr_b16 a[172:173], v11 offset:35840
s_add_u32 s68, s68, 2048
ds_read_b64_tr_b16 a[174:175], v11 offset:36864
s_add_u32 m0, m0, 1024
s_add_u32 s6, s6, 28672
s_cmp_ge_u32 s6, 1605632
s_cselect_b32 s7, 1605632, 0
s_sub_u32 s6, s6, s7
buffer_load_dwordx4 v33, s[24:27], s6 offen lds
ds_read_b64_tr_b16 a[176:177], v8 offset:38912
ds_read_b64_tr_b16 a[178:179], v8 offset:39936
s_and_b32 s68, s68, 0x3fff
ds_read_b64_tr_b16 a[180:181], v9 offset:38912
ds_read_b64_tr_b16 a[182:183], v9 offset:39936
ds_read_b64_tr_b16 a[184:185], v10 offset:38912
s_add_u32 s93, s67, s68
ds_read_b64_tr_b16 a[186:187], v10 offset:39936
s_add_u32 m0, m0, 1024
s_add_u32 s6, s6, 28672
s_cmp_ge_u32 s6, 1605632
s_cselect_b32 s7, 1605632, 0
s_sub_u32 s6, s6, s7
buffer_load_dwordx4 v33, s[24:27], s6 offen lds
ds_read_b64_tr_b16 a[188:189], v11 offset:38912
ds_read_b64_tr_b16 a[190:191], v11 offset:39936
s_add_u32 s68, s68, 2048
ds_read_b64_tr_b16 a[192:193], v8 offset:43008
ds_read_b64_tr_b16 a[194:195], v8 offset:44032
ds_read_b64_tr_b16 a[196:197], v9 offset:43008
s_and_b32 s68, s68, 0x3fff
s_add_u32 m0, m0, 1024
s_add_u32 s6, s6, 28672
s_cmp_ge_u32 s6, 1605632
s_cselect_b32 s7, 1605632, 0
s_sub_u32 s6, s6, s7
buffer_load_dwordx4 v33, s[24:27], s6 offen lds
ds_read_b64_tr_b16 a[198:199], v9 offset:44032
ds_read_b64_tr_b16 a[200:201], v10 offset:43008
ds_read_b64_tr_b16 a[202:203], v10 offset:44032
s_add_u32 s94, s67, s68
ds_read_b64_tr_b16 a[204:205], v11 offset:43008
ds_read_b64_tr_b16 a[206:207], v11 offset:44032
ds_read_b64_tr_b16 a[208:209], v8 offset:46080
s_add_u32 s68, s68, 2048
s_add_u32 m0, m0, 1024
s_add_u32 s6, s6, 28672
s_cmp_ge_u32 s6, 1605632
s_cselect_b32 s7, 1605632, 0
s_sub_u32 s6, s6, s7
buffer_load_dwordx4 v33, s[24:27], s6 offen lds
ds_read_b64_tr_b16 a[210:211], v8 offset:47104
ds_read_b64_tr_b16 a[212:213], v9 offset:46080
ds_read_b64_tr_b16 a[214:215], v9 offset:47104
s_and_b32 s68, s68, 0x3fff
ds_read_b64_tr_b16 a[216:217], v10 offset:46080
ds_read_b64_tr_b16 a[218:219], v10 offset:47104
ds_read_b64_tr_b16 a[220:221], v11 offset:46080
s_add_u32 s95, s67, s68
s_add_u32 m0, m0, 1024
s_add_u32 s6, s6, 28672
s_cmp_ge_u32 s6, 1605632
s_cselect_b32 s7, 1605632, 0
s_sub_u32 s6, s6, s7
buffer_load_dwordx4 v33, s[24:27], s6 offen lds
ds_read_b64_tr_b16 a[222:223], v11 offset:47104
ds_read_b64_tr_b16 a[224:225], v8 offset:50176
ds_read_b64_tr_b16 a[226:227], v8 offset:51200
s_add_u32 s68, s68, 2048
ds_read_b64_tr_b16 a[228:229], v9 offset:50176
ds_read_b64_tr_b16 a[230:231], v9 offset:51200
ds_read_b64_tr_b16 a[232:233], v10 offset:50176
s_add_u32 m0, m0, 1024
s_add_u32 s6, s6, 28672
s_cmp_ge_u32 s6, 1605632
s_cselect_b32 s7, 1605632, 0
s_sub_u32 s6, s6, s7
buffer_load_dwordx4 v33, s[24:27], s6 offen lds
s_and_b32 s68, s68, 0x3fff
ds_read_b64_tr_b16 a[234:235], v10 offset:51200
ds_read_b64_tr_b16 a[236:237], v11 offset:50176
ds_read_b64_tr_b16 a[238:239], v11 offset:51200
s_add_u32 s96, s67, s68
ds_read_b64_tr_b16 a[240:241], v8 offset:53248
ds_read_b64_tr_b16 a[242:243], v8 offset:54272
ds_read_b64_tr_b16 a[244:245], v9 offset:53248
s_add_u32 m0, m0, 1024
s_add_u32 s6, s6, 28672
s_cmp_ge_u32 s6, 1605632
s_cselect_b32 s7, 1605632, 0
s_sub_u32 s6, s6, s7
buffer_load_dwordx4 v33, s[24:27], s6 offen lds
s_add_u32 s68, s68, 2048
ds_read_b64_tr_b16 a[246:247], v9 offset:54272
ds_read_b64_tr_b16 a[248:249], v10 offset:53248
ds_read_b64_tr_b16 a[250:251], v10 offset:54272
s_and_b32 s68, s68, 0x3fff
ds_read_b64_tr_b16 a[252:253], v11 offset:53248
ds_read_b64_tr_b16 a[254:255], v11 offset:54272
s_waitcnt lgkmcnt(0)
s_and_b32 exec_lo, 1, s3
s_mov_b32 exec_hi, 0
s_mov_b32 s66, 32768
.Lk_poll_5:
buffer_load_dword v2, off, s[16:19], s63 sc1
s_waitcnt vmcnt(0)
v_cmp_ne_u32_e32 vcc, s62, v2
s_nop 4
s_cbranch_vccz .Lk_done_6
s_sub_u32 s66, s66, 1
s_cbranch_scc0 .Lk_poll_5
.Lk_done_6:
s_mov_b64 exec, -1
s_barrier
buffer_load_dwordx4 v[112:115], v1, s[16:19], s89 offen sc1
buffer_load_dwordx4 v[116:119], v1, s[16:19], s89 offen offset:1024 sc1
buffer_load_dwordx4 v[120:123], v1, s[16:19], s90 offen sc1
buffer_load_dwordx4 v[124:127], v1, s[16:19], s90 offen offset:1024 sc1
buffer_load_dwordx4 v[128:131], v1, s[16:19], s91 offen sc1
buffer_load_dwordx4 v[132:135], v1, s[16:19], s91 offen offset:1024 sc1
buffer_load_dwordx4 v[136:139], v1, s[16:19], s92 offen sc1
buffer_load_dwordx4 v[140:143], v1, s[16:19], s92 offen offset:1024 sc1
s_lshr_b32 s68, s54, 1
s_mul_i32 s68, s68, 28
s_lshl2_add_u32 s68, s65, s68
s_lshl_b32 s68, s68, 10
s_and_b32 s69, s49, 1
s_lshl_b32 s69, s69, 8
s_add_u32 s68, s68, s69
s_add_u32 s68, s68, 6291456
s_lshl3_add_u32 s69, s54, s65
s_lshl_b32 s69, s69, 7
s_add_u32 s69, s69, 1572864
s_lshr_b32 s67, s54, 3
s_lshl_b32 s67, s67, 7
s_add_u32 s67, s67, 1048576
v_lshlrev_b32 v48, 16, v34
v_and_b32 v49, 0xffff0000, v34
v_lshlrev_b32 v50, 16, v35
v_and_b32 v51, 0xffff0000, v35
v_lshlrev_b32 v52, 16, v36
v_and_b32 v53, 0xffff0000, v36
v_lshlrev_b32 v54, 16, v37
v_and_b32 v55, 0xffff0000, v37
v_lshlrev_b32 v56, 16, v38
v_and_b32 v57, 0xffff0000, v38
v_lshlrev_b32 v58, 16, v39
v_and_b32 v59, 0xffff0000, v39
v_lshlrev_b32 v60, 16, v40
v_and_b32 v61, 0xffff0000, v40
v_lshlrev_b32 v62, 16, v41
v_and_b32 v63, 0xffff0000, v41
s_waitcnt vmcnt(7)
v_mfma_f32_16x16x32_bf16 v[48:51], a[0:3], v[112:115], v[48:51]
buffer_load_dwordx4 v[144:147], v1, s[16:19], s93 offen sc1
v_mfma_f32_16x16x32_bf16 v[52:55], a[4:7], v[112:115], v[52:55]
v_mfma_f32_16x16x32_bf16 v[56:59], a[8:11], v[112:115], v[56:59]
v_mfma_f32_16x16x32_bf16 v[60:63], a[12:15], v[112:115], v[60:63]
s_waitcnt vmcnt(7)
v_mfma_f32_16x16x32_bf16 v[48:51], a[16:19], v[116:119], v[48:51]
buffer_load_dwordx4 v[148:151], v1, s[16:19], s93 offen offset:1024 sc1
v_mfma_f32_16x16x32_bf16 v[52:55], a[20:23], v[116:119], v[52:55]
v_mfma_f32_16x16x32_bf16 v[56:59], a[24:27], v[116:119], v[56:59]
v_mfma_f32_16x16x32_bf16 v[60:63], a[28:31], v[116:119], v[60:63]
s_waitcnt vmcnt(7)
v_mfma_f32_16x16x32_bf16 v[48:51], a[32:35], v[120:123], v[48:51]
buffer_load_dwordx4 v[152:155], v1, s[16:19], s94 offen sc1
v_mfma_f32_16x16x32_bf16 v[52:55], a[36:39], v[120:123], v[52:55]
v_mfma_f32_16x16x32_bf16 v[56:59], a[40:43], v[120:123], v[56:59]
v_mfma_f32_16x16x32_bf16 v[60:63], a[44:47], v[120:123], v[60:63]
s_waitcnt vmcnt(7)
v_mfma_f32_16x16x32_bf16 v[48:51], a[48:51], v[124:127], v[48:51]
buffer_load_dwordx4 v[156:159], v1, s[16:19], s94 offen offset:1024 sc1
v_mfma_f32_16x16x32_bf16 v[52:55], a[52:55], v[124:127], v[52:55]
v_mfma_f32_16x16x32_bf16 v[56:59], a[56:59], v[124:127], v[56:59]
v_mfma_f32_16x16x32_bf16 v[60:63], a[60:63], v[124:127], v[60:63]
s_waitcnt vmcnt(7)
v_mfma_f32_16x16x32_bf16 v[48:51], a[64:67], v[128:131], v[48:51]
buffer_load_dwordx4 v[160:163], v1, s[16:19], s95 offen sc1
v_mfma_f32_16x16x32_bf16 v[52:55], a[68:71], v[128:131], v[52:55]
v_mfma_f32_16x16x32_bf16 v[56:59], a[72:75], v[128:131], v[56:59]
v_mfma_f32_16x16x32_bf16 v[60:63], a[76:79], v[128:131], v[60:63]
s_waitcnt vmcnt(7)
v_mfma_f32_16x16x32_bf16 v[48:51], a[80:83], v[132:135], v[48:51]
buffer_load_dwordx4 v[164:167], v1, s[16:19], s95 offen offset:1024 sc1
v_mfma_f32_16x16x32_bf16 v[52:55], a[84:87], v[132:135], v[52:55]
v_mfma_f32_16x16x32_bf16 v[56:59], a[88:91], v[132:135], v[56:59]
v_mfma_f32_16x16x32_bf16 v[60:63], a[92:95], v[132:135], v[60:63]
s_waitcnt vmcnt(7)
v_mfma_f32_16x16x32_bf16 v[48:51], a[96:99], v[136:139], v[48:51]
buffer_load_dwordx4 v[168:171], v1, s[16:19], s96 offen sc1
v_mfma_f32_16x16x32_bf16 v[52:55], a[100:103], v[136:139], v[52:55]
v_mfma_f32_16x16x32_bf16 v[56:59], a[104:107], v[136:139], v[56:59]
v_mfma_f32_16x16x32_bf16 v[60:63], a[108:111], v[136:139], v[60:63]
s_waitcnt vmcnt(7)
v_mfma_f32_16x16x32_bf16 v[48:51], a[112:115], v[140:143], v[48:51]
buffer_load_dwordx4 v[172:175], v1, s[16:19], s96 offen offset:1024 sc1
v_mfma_f32_16x16x32_bf16 v[52:55], a[116:119], v[140:143], v[52:55]
v_mfma_f32_16x16x32_bf16 v[56:59], a[120:123], v[140:143], v[56:59]
v_mfma_f32_16x16x32_bf16 v[60:63], a[124:127], v[140:143], v[60:63]
s_waitcnt vmcnt(7)
v_mfma_f32_16x16x32_bf16 v[48:51], a[128:131], v[144:147], v[48:51]
s_and_b32 exec_lo, 0xffff0000, s3
s_mov_b32 exec_hi, 0
buffer_load_dword v21, v23, s[16:19], 0 offen sc1
s_mov_b64 exec, -1
v_mfma_f32_16x16x32_bf16 v[52:55], a[132:135], v[144:147], v[52:55]
v_mfma_f32_16x16x32_bf16 v[56:59], a[136:139], v[144:147], v[56:59]
v_mfma_f32_16x16x32_bf16 v[60:63], a[140:143], v[144:147], v[60:63]
s_waitcnt vmcnt(7)
v_mfma_f32_16x16x32_bf16 v[48:51], a[144:147], v[148:151], v[48:51]
v_mfma_f32_16x16x32_bf16 v[52:55], a[148:151], v[148:151], v[52:55]
v_mfma_f32_16x16x32_bf16 v[56:59], a[152:155], v[148:151], v[56:59]
v_mfma_f32_16x16x32_bf16 v[60:63], a[156:159], v[148:151], v[60:63]
s_waitcnt vmcnt(6)
v_mfma_f32_16x16x32_bf16 v[48:51], a[160:163], v[152:155], v[48:51]
v_mfma_f32_16x16x32_bf16 v[52:55], a[164:167], v[152:155], v[52:55]
v_mfma_f32_16x16x32_bf16 v[56:59], a[168:171], v[152:155], v[56:59]
v_mfma_f32_16x16x32_bf16 v[60:63], a[172:175], v[152:155], v[60:63]
s_waitcnt vmcnt(5)
v_mfma_f32_16x16x32_bf16 v[48:51], a[176:179], v[156:159], v[48:51]
v_mfma_f32_16x16x32_bf16 v[52:55], a[180:183], v[156:159], v[52:55]
v_mfma_f32_16x16x32_bf16 v[56:59], a[184:187], v[156:159], v[56:59]
v_mfma_f32_16x16x32_bf16 v[60:63], a[188:191], v[156:159], v[60:63]
s_waitcnt vmcnt(4)
v_mfma_f32_16x16x32_bf16 v[48:51], a[192:195], v[160:163], v[48:51]
v_mfma_f32_16x16x32_bf16 v[52:55], a[196:199], v[160:163], v[52:55]
v_mfma_f32_16x16x32_bf16 v[56:59], a[200:203], v[160:163], v[56:59]
v_mfma_f32_16x16x32_bf16 v[60:63], a[204:207], v[160:163], v[60:63]
s_waitcnt vmcnt(3)
v_mfma_f32_16x16x32_bf16 v[48:51], a[208:211], v[164:167], v[48:51]
v_mfma_f32_16x16x32_bf16 v[52:55], a[212:215], v[164:167], v[52:55]
v_mfma_f32_16x16x32_bf16 v[56:59], a[216:219], v[164:167], v[56:59]
v_mfma_f32_16x16x32_bf16 v[60:63], a[220:223], v[164:167], v[60:63]
s_waitcnt vmcnt(2)
v_mfma_f32_16x16x32_bf16 v[48:51], a[224:227], v[168:171], v[48:51]
v_mfma_f32_16x16x32_bf16 v[52:55], a[228:231], v[168:171], v[52:55]
v_mfma_f32_16x16x32_bf16 v[56:59], a[232:235], v[168:171], v[56:59]
v_mfma_f32_16x16x32_bf16 v[60:63], a[236:239], v[168:171], v[60:63]
s_waitcnt vmcnt(1)
v_mfma_f32_16x16x32_bf16 v[48:51], a[240:243], v[172:175], v[48:51]
v_mfma_f32_16x16x32_bf16 v[52:55], a[244:247], v[172:175], v[52:55]
v_mfma_f32_16x16x32_bf16 v[56:59], a[248:251], v[172:175], v[56:59]
v_mfma_f32_16x16x32_bf16 v[60:63], a[252:255], v[172:175], v[60:63]
s_nop 4
v_cvt_pk_bf16_f32 v24, v48, v49
v_cvt_pk_bf16_f32 v25, v50, v51
v_cvt_pk_bf16_f32 v26, v52, v53
v_cvt_pk_bf16_f32 v27, v54, v55
buffer_store_dwordx4 v[24:27], v12, s[16:19], s68 offen sc1
v_cvt_pk_bf16_f32 v28, v56, v57
v_cvt_pk_bf16_f32 v29, v58, v59
v_cvt_pk_bf16_f32 v30, v60, v61
v_cvt_pk_bf16_f32 v31, v62, v63
buffer_store_dwordx4 v[28:31], v12, s[16:19], s68 offen offset:2048 sc1
v_dot2c_f32_bf16 v14, s73, v24
v_dot2c_f32_bf16 v15, v24, v24
v_dot2c_f32_bf16 v14, s73, v25
v_dot2c_f32_bf16 v15, v25, v25
v_dot2c_f32_bf16 v14, s73, v26
v_dot2c_f32_bf16 v15, v26, v26
v_dot2c_f32_bf16 v14, s73, v27
v_dot2c_f32_bf16 v15, v27, v27
v_dot2c_f32_bf16 v14, s73, v28
v_dot2c_f32_bf16 v15, v28, v28
v_dot2c_f32_bf16 v14, s73, v29
v_dot2c_f32_bf16 v15, v29, v29
v_dot2c_f32_bf16 v14, s73, v30
v_dot2c_f32_bf16 v15, v30, v30
v_dot2c_f32_bf16 v14, s73, v31
v_dot2c_f32_bf16 v15, v31, v31
s_nop 2
v_permlane32_swap_b32 v14, v15
v_add_f32 v14, v14, v15
v_mov_b32 v15, v14
s_nop 1
v_permlane16_swap_b32 v14, v15
v_add_f32 v14, v14, v15
s_mov_b32 exec_lo, 0xffff
s_mov_b32 exec_hi, 0xffff
buffer_store_dword v14, v13, s[16:19], s69 offen sc1
s_mov_b64 exec, -1
v_lshrrev_b32 v48, 5, v0
v_bfe_u32 v49, v0, 2, 2
v_lshl_add_u32 v48, v48, 2, v49
v_lshlrev_b32 v48, 7, v48
v_bfe_u32 v49, v0, 3, 1
v_lshl_add_u32 v48, v49, 6, v48
v_bfe_u32 v49, v0, 4, 1
v_lshl_add_u32 v48, v49, 5, v48
v_and_b32 v49, 3, v0
v_lshl_add_u32 v48, v49, 3, v48
v_add_u32 v8, 57344, v48
v_xor_b32 v9, 64, v8
v_subrev_u32 v16, 57344, v8
v_subrev_u32 v17, 57344, v9
s_waitcnt vmcnt(0)
s_barrier
s_and_b32 exec_lo, 1, s3
s_mov_b32 exec_hi, 0
buffer_atomic_add v3, off, s[16:19], s67
s_mov_b64 exec, -1
v_xor_b32 v48, v21, v22
v_and_b32 v48, 1, v48
v_cmp_ne_u32_e64 s[76:77], 0, v48
s_nop 0
s_and_b32 s76, s76, 0xffff0000
s_and_b32 s76, s76, s3
s_cbranch_scc0 .Lk_fresh_7
v_and_b32 v48, 1, v21
v_lshl_add_u32 v48, v48, 7, v23
s_mov_b32 exec_lo, s76
s_mov_b32 exec_hi, 0
buffer_atomic_add v3, v48, s[16:19], 0 offen offset:384
s_waitcnt vmcnt(0)
s_mov_b64 exec, -1
.Lk_fresh_7:
s_mul_i32 s6, s49, 14
s_lshl_b32 s7, s6, 10
s_add_u32 m0, s7, 0
s_mul_i32 s7, s85, 8
s_add_u32 s6, s6, s7
s_add_u32 s6, s6, 0
s_mul_i32 s6, s6, 28672
s_cmp_ge_u32 s6, 1605632
s_cselect_b32 s7, 1605632, 0
s_sub_u32 s6, s6, s7
buffer_load_dwordx4 v33, s[24:27], s6 offen lds
s_mov_b32 s80, 0xbdd2d3e8
ds_read_b64_tr_b16 a[0:1], v8 offset:0
ds_read_b64_tr_b16 a[2:3], v8 offset:2048
ds_read_b64_tr_b16 a[4:5], v9 offset:0
s_mov_b32 s81, 0xbdd2d3e8
ds_read_b64_tr_b16 a[6:7], v9 offset:2048
ds_read_b64_tr_b16 a[8:9], v8 offset:1024
ds_read_b64_tr_b16 a[10:11], v8 offset:3072
v_mov_b32 v10, 0xc0135761
ds_read_b64_tr_b16 a[12:13], v9 offset:1024
ds_read_b64_tr_b16 a[14:15], v9 offset:3072
s_add_u32 m0, m0, 1024
s_add_u32 s6, s6, 28672
s_cmp_ge_u32 s6, 1605632
s_cselect_b32 s7, 1605632, 0
s_sub_u32 s6, s6, s7
buffer_load_dwordx4 v33, s[24:27], s6 offen lds
ds_read_b64_tr_b16 a[16:17], v8 offset:4096
s_lshl2_add_u32 s55, s50, s49
ds_read_b64_tr_b16 a[18:19], v8 offset:6144
ds_read_b64_tr_b16 a[20:21], v9 offset:4096
s_mul_i32 s67, s55, 28672
ds_read_b64_tr_b16 a[22:23], v9 offset:6144
ds_read_b64_tr_b16 a[24:25], v8 offset:5120
ds_read_b64_tr_b16 a[26:27], v8 offset:7168
s_add_u32 s67, s67, 6291456
ds_read_b64_tr_b16 a[28:29], v9 offset:5120
ds_read_b64_tr_b16 a[30:31], v9 offset:7168
s_add_u32 m0, m0, 1024
s_add_u32 s6, s6, 28672
s_cmp_ge_u32 s6, 1605632
s_cselect_b32 s7, 1605632, 0
s_sub_u32 s6, s6, s7
buffer_load_dwordx4 v33, s[24:27], s6 offen lds
ds_read_b64_tr_b16 a[32:33], v8 offset:8192
s_lshl_b32 s68, s85, 12
ds_read_b64_tr_b16 a[34:35], v8 offset:10240
ds_read_b64_tr_b16 a[36:37], v9 offset:8192
ds_read_b64_tr_b16 a[38:39], v9 offset:10240
s_add_u32 s89, s67, s68
ds_read_b64_tr_b16 a[40:41], v8 offset:9216
ds_read_b64_tr_b16 a[42:43], v8 offset:11264
s_add_u32 s68, s68, 4096
ds_read_b64_tr_b16 a[44:45], v9 offset:9216
ds_read_b64_tr_b16 a[46:47], v9 offset:11264
s_add_u32 m0, m0, 1024
s_add_u32 s6, s6, 28672
s_cmp_ge_u32 s6, 1605632
s_cselect_b32 s7, 1605632, 0
s_sub_u32 s6, s6, s7
buffer_load_dwordx4 v33, s[24:27], s6 offen lds
ds_read_b64_tr_b16 a[48:49], v8 offset:12288
s_cmp_eq_u32 s68, 28672
s_cselect_b32 s68, 0, s68
ds_read_b64_tr_b16 a[50:51], v8 offset:14336
ds_read_b64_tr_b16 a[52:53], v9 offset:12288
ds_read_b64_tr_b16 a[54:55], v9 offset:14336
s_add_u32 s90, s67, s68
ds_read_b64_tr_b16 a[56:57], v8 offset:13312
ds_read_b64_tr_b16 a[58:59], v8 offset:15360
ds_read_b64_tr_b16 a[60:61], v9 offset:13312
s_add_u32 s68, s68, 4096
ds_read_b64_tr_b16 a[62:63], v9 offset:15360
s_add_u32 m0, m0, 1024
s_add_u32 s6, s6, 28672
s_cmp_ge_u32 s6, 1605632
s_cselect_b32 s7, 1605632, 0
s_sub_u32 s6, s6, s7
buffer_load_dwordx4 v33, s[24:27], s6 offen lds
ds_read_b64_tr_b16 a[64:65], v8 offset:16384
s_cmp_eq_u32 s68, 28672
s_cselect_b32 s68, 0, s68
ds_read_b64_tr_b16 a[66:67], v8 offset:18432
ds_read_b64_tr_b16 a[68:69], v9 offset:16384
ds_read_b64_tr_b16 a[70:71], v9 offset:18432
s_add_u32 s91, s67, s68
ds_read_b64_tr_b16 a[72:73], v8 offset:17408
ds_read_b64_tr_b16 a[74:75], v8 offset:19456
ds_read_b64_tr_b16 a[76:77], v9 offset:17408
s_add_u32 s68, s68, 4096
ds_read_b64_tr_b16 a[78:79], v9 offset:19456
s_add_u32 m0, m0, 1024
s_add_u32 s6, s6, 28672
s_cmp_ge_u32 s6, 1605632
s_cselect_b32 s7, 1605632, 0
s_sub_u32 s6, s6, s7
buffer_load_dwordx4 v33, s[24:27], s6 offen lds
ds_read_b64_tr_b16 a[80:81], v8 offset:20480
s_cmp_eq_u32 s68, 28672
s_cselect_b32 s68, 0, s68
ds_read_b64_tr_b16 a[82:83], v8 offset:22528
ds_read_b64_tr_b16 a[84:85], v9 offset:20480
ds_read_b64_tr_b16 a[86:87], v9 offset:22528
s_add_u32 s92, s67, s68
ds_read_b64_tr_b16 a[88:89], v8 offset:21504
ds_read_b64_tr_b16 a[90:91], v8 offset:23552
ds_read_b64_tr_b16 a[92:93], v9 offset:21504
s_add_u32 s68, s68, 4096
ds_read_b64_tr_b16 a[94:95], v9 offset:23552
s_add_u32 m0, m0, 1024
s_add_u32 s6, s6, 28672
s_cmp_ge_u32 s6, 1605632
s_cselect_b32 s7, 1605632, 0
s_sub_u32 s6, s6, s7
buffer_load_dwordx4 v33, s[24:27], s6 offen lds
ds_read_b64_tr_b16 a[96:97], v8 offset:24576
ds_read_b64_tr_b16 a[98:99], v8 offset:26624
s_cmp_eq_u32 s68, 28672
s_cselect_b32 s68, 0, s68
ds_read_b64_tr_b16 a[100:101], v9 offset:24576
ds_read_b64_tr_b16 a[102:103], v9 offset:26624
s_add_u32 s93, s67, s68
ds_read_b64_tr_b16 a[104:105], v8 offset:25600
ds_read_b64_tr_b16 a[106:107], v8 offset:27648
ds_read_b64_tr_b16 a[108:109], v9 offset:25600
s_add_u32 s68, s68, 4096
ds_read_b64_tr_b16 a[110:111], v9 offset:27648
s_add_u32 m0, m0, 1024
s_add_u32 s6, s6, 28672
s_cmp_ge_u32 s6, 1605632
s_cselect_b32 s7, 1605632, 0
s_sub_u32 s6, s6, s7
buffer_load_dwordx4 v33, s[24:27], s6 offen lds
ds_read_b64_tr_b16 a[112:113], v8 offset:28672
ds_read_b64_tr_b16 a[114:115], v8 offset:30720
s_cmp_eq_u32 s68, 28672
s_cselect_b32 s68, 0, s68
ds_read_b64_tr_b16 a[116:117], v9 offset:28672
ds_read_b64_tr_b16 a[118:119], v9 offset:30720
ds_read_b64_tr_b16 a[120:121], v8 offset:29696
s_add_u32 s94, s67, s68
ds_read_b64_tr_b16 a[122:123], v8 offset:31744
ds_read_b64_tr_b16 a[124:125], v9 offset:29696
s_add_u32 s68, s68, 4096
ds_read_b64_tr_b16 a[126:127], v9 offset:31744
s_add_u32 m0, m0, 1024
s_add_u32 s6, s6, 28672
s_cmp_ge_u32 s6, 1605632
s_cselect_b32 s7, 1605632, 0
s_sub_u32 s6, s6, s7
buffer_load_dwordx4 v33, s[24:27], s6 offen lds
ds_read_b64_tr_b16 a[128:129], v8 offset:32768
ds_read_b64_tr_b16 a[130:131], v8 offset:34816
s_cmp_eq_u32 s68, 28672
s_cselect_b32 s68, 0, s68
ds_read_b64_tr_b16 a[132:133], v9 offset:32768
ds_read_b64_tr_b16 a[134:135], v9 offset:34816
ds_read_b64_tr_b16 a[136:137], v8 offset:33792
s_add_u32 s95, s67, s68
ds_read_b64_tr_b16 a[138:139], v8 offset:35840
ds_read_b64_tr_b16 a[140:141], v9 offset:33792
ds_read_b64_tr_b16 a[142:143], v9 offset:35840
s_add_u32 s68, s68, 4096
s_add_u32 m0, m0, 1024
s_add_u32 s6, s6, 28672
s_cmp_ge_u32 s6, 1605632
s_cselect_b32 s7, 1605632, 0
s_sub_u32 s6, s6, s7
buffer_load_dwordx4 v33, s[24:27], s6 offen lds
ds_read_b64_tr_b16 a[144:145], v8 offset:36864
ds_read_b64_tr_b16 a[146:147], v8 offset:38912
s_cmp_eq_u32 s68, 28672
s_cselect_b32 s68, 0, s68
ds_read_b64_tr_b16 a[148:149], v9 offset:36864
ds_read_b64_tr_b16 a[150:151], v9 offset:38912
ds_read_b64_tr_b16 a[152:153], v8 offset:37888
v_bfe_u32 v49, v0, 4, 1
ds_read_b64_tr_b16 a[154:155], v8 offset:39936
ds_read_b64_tr_b16 a[156:157], v9 offset:37888
ds_read_b64_tr_b16 a[158:159], v9 offset:39936
v_lshlrev_b32 v49, 10, v49
s_add_u32 m0, m0, 1024
s_add_u32 s6, s6, 28672
s_cmp_ge_u32 s6, 1605632
s_cselect_b32 s7, 1605632, 0
s_sub_u32 s6, s6, s7
buffer_load_dwordx4 v33, s[24:27], s6 offen lds
ds_read_b64_tr_b16 a[160:161], v8 offset:40960
ds_read_b64_tr_b16 a[162:163], v8 offset:43008
v_lshrrev_b32 v50, 5, v0
ds_read_b64_tr_b16 a[164:165], v9 offset:40960
ds_read_b64_tr_b16 a[166:167], v9 offset:43008
ds_read_b64_tr_b16 a[168:169], v8 offset:41984
v_lshl_add_u32 v49, v50, 6, v49
ds_read_b64_tr_b16 a[170:171], v8 offset:44032
ds_read_b64_tr_b16 a[172:173], v9 offset:41984
ds_read_b64_tr_b16 a[174:175], v9 offset:44032
v_and_b32 v50, 15, v0
s_add_u32 m0, m0, 1024
s_add_u32 s6, s6, 28672
s_cmp_ge_u32 s6, 1605632
s_cselect_b32 s7, 1605632, 0
s_sub_u32 s6, s6, s7
buffer_load_dwordx4 v33, s[24:27], s6 offen lds
ds_read_b64_tr_b16 a[176:177], v8 offset:45056
ds_read_b64_tr_b16 a[178:179], v8 offset:47104
ds_read_b64_tr_b16 a[180:181], v9 offset:45056
v_lshl_add_u32 v49, v50, 2, v49
ds_read_b64_tr_b16 a[182:183], v9 offset:47104
ds_read_b64_tr_b16 a[184:185], v8 offset:46080
s_lshl_b32 s68, s55, 11
ds_read_b64_tr_b16 a[186:187], v8 offset:48128
ds_read_b64_tr_b16 a[188:189], v9 offset:46080
ds_read_b64_tr_b16 a[190:191], v9 offset:48128
s_add_u32 s68, s68, 1572864
s_add_u32 m0, m0, 1024
s_add_u32 s6, s6, 28672
s_cmp_ge_u32 s6, 1605632
s_cselect_b32 s7, 1605632, 0
s_sub_u32 s6, s6, s7
buffer_load_dwordx4 v33, s[24:27], s6 offen lds
ds_read_b64_tr_b16 a[192:193], v8 offset:49152
ds_read_b64_tr_b16 a[194:195], v8 offset:51200
ds_read_b64_tr_b16 a[196:197], v9 offset:49152
s_lshl_b32 s69, s51, 8
ds_read_b64_tr_b16 a[198:199], v9 offset:51200
ds_read_b64_tr_b16 a[200:201], v8 offset:50176
ds_read_b64_tr_b16 a[202:203], v8 offset:52224
s_add_u32 s69, s69, 1703936
ds_read_b64_tr_b16 a[204:205], v9 offset:50176
ds_read_b64_tr_b16 a[206:207], v9 offset:52224
v_lshlrev_b32 v21, 2, v0
s_add_u32 m0, m0, 1024
s_add_u32 s6, s6, 28672
s_cmp_ge_u32 s6, 1605632
s_cselect_b32 s7, 1605632, 0
s_sub_u32 s6, s6, s7
buffer_load_dwordx4 v33, s[24:27], s6 offen lds
ds_read_b64_tr_b16 a[208:209], v8 offset:53248
ds_read_b64_tr_b16 a[210:211], v8 offset:55296
ds_read_b64_tr_b16 a[212:213], v9 offset:53248
s_lshl_b32 s67, s50, 7
ds_read_b64_tr_b16 a[214:215], v9 offset:55296
ds_read_b64_tr_b16 a[216:217], v8 offset:54272
ds_read_b64_tr_b16 a[218:219], v8 offset:56320
s_add_u32 s67, s67, 1048576
ds_read_b64_tr_b16 a[220:221], v9 offset:54272
ds_read_b64_tr_b16 a[222:223], v9 offset:56320
buffer_load_dword v19, v21, s[16:19], s69 offen sc1
buffer_load_dword v11, v21, s[16:19], s69 offen offset:3584 sc1
s_waitcnt lgkmcnt(0)
s_and_b32 exec_lo, 1, s3
s_mov_b32 exec_hi, 0
s_mov_b32 s66, 32768
.Lk_poll_8:
buffer_load_dword v2, off, s[16:19], s67 sc1
s_waitcnt vmcnt(0)
v_cmp_ne_u32_e32 vcc, 14, v2
s_nop 4
s_cbranch_vccz .Lk_done_9
s_sub_u32 s66, s66, 1
s_cbranch_scc0 .Lk_poll_8
.Lk_done_9:
s_mov_b64 exec, -1
s_barrier
buffer_load_dwordx4 v[112:115], v1, s[16:19], s89 offen sc1
buffer_load_dwordx4 v[116:119], v1, s[16:19], s89 offen offset:1024 sc1
buffer_load_dwordx4 v[120:123], v1, s[16:19], s89 offen offset:2048 sc1
buffer_load_dwordx4 v[124:127], v1, s[16:19], s89 offen offset:3072 sc1
buffer_load_dword v80, v49, s[16:19], s68 offen sc1
buffer_load_dword v81, v49, s[16:19], s68 offen offset:128 sc1
buffer_load_dword v82, v49, s[16:19], s68 offen offset:256 sc1
buffer_load_dword v83, v49, s[16:19], s68 offen offset:384 sc1
buffer_load_dword v84, v49, s[16:19], s68 offen offset:512 sc1
buffer_load_dword v85, v49, s[16:19], s68 offen offset:640 sc1
buffer_load_dword v86, v49, s[16:19], s68 offen offset:768 sc1
buffer_load_dwordx4 v[128:131], v1, s[16:19], s90 offen sc1
buffer_load_dwordx4 v[132:135], v1, s[16:19], s90 offen offset:1024 sc1
buffer_load_dwordx4 v[136:139], v1, s[16:19], s90 offen offset:2048 sc1
buffer_load_dwordx4 v[140:143], v1, s[16:19], s90 offen offset:3072 sc1
v_max_u32 v50, v19, v11
v_cmp_eq_u32_e32 vcc, -1, v50
s_nop 4
s_cbranch_vccz .Lk_csdone_11
s_mov_b32 s66, 32768
.Lk_cs_10:
buffer_load_dword v19, v21, s[16:19], s69 offen sc1
buffer_load_dword v11, v21, s[16:19], s69 offen offset:3584 sc1
s_waitcnt vmcnt(0)
v_max_u32 v50, v19, v11
v_cmp_eq_u32_e32 vcc, -1, v50
s_nop 4
s_cbranch_vccz .Lk_csdone_11
s_sub_u32 s66, s66, 1
s_cbranch_scc0 .Lk_cs_10
.Lk_csdone_11:
v_cndmask_b32_e64 v56, v11, 0, s[74:75]
v_mov_b32 v58, 0
v_mov_b32 v57, v56
v_mov_b32 v59, 0
v_cndmask_b32_e64 v60, 0, v11, s[74:75]
v_mov_b32 v62, 0
v_mov_b32 v61, v60
v_mov_b32 v63, 0
s_lshl_b32 s67, s55, 12
s_add_u32 s56, s67, 1310720
s_lshl_b32 s68, s51, 8
s_add_u32 s68, s68, s56
s_waitcnt vmcnt(14)
v_mfma_f32_32x32x16_bf16 v[224:239], a[0:3], v[112:115], 0
buffer_load_dwordx4 v[144:147], v1, s[16:19], s91 offen sc1
v_mfma_f32_32x32x16_bf16 v[240:255], a[4:7], v[112:115], 0
s_waitcnt vmcnt(14)
v_mfma_f32_32x32x16_bf16 v[224:239], a[8:11], v[116:119], v[224:239]
ds_read_b64_tr_b16 a[0:1], v16 offset:0
ds_read_b64_tr_b16 a[2:3], v16 offset:2048
buffer_load_dwordx4 v[148:151], v1, s[16:19], s91 offen offset:1024 sc1
v_mfma_f32_32x32x16_bf16 v[240:255], a[12:15], v[116:119], v[240:255]
ds_read_b64_tr_b16 a[4:5], v17 offset:0
ds_read_b64_tr_b16 a[6:7], v17 offset:2048
s_waitcnt vmcnt(14)
v_mfma_f32_32x32x16_bf16 v[224:239], a[16:19], v[120:123], v[224:239]
ds_read_b64_tr_b16 a[8:9], v16 offset:1024
ds_read_b64_tr_b16 a[10:11], v16 offset:3072
buffer_load_dwordx4 v[152:155], v1, s[16:19], s91 offen offset:2048 sc1
v_mfma_f32_32x32x16_bf16 v[240:255], a[20:23], v[120:123], v[240:255]
ds_read_b64_tr_b16 a[12:13], v17 offset:1024
ds_read_b64_tr_b16 a[14:15], v17 offset:3072
s_waitcnt vmcnt(14)
v_mfma_f32_32x32x16_bf16 v[224:239], a[24:27], v[124:127], v[224:239]
ds_read_b64_tr_b16 a[16:17], v16 offset:4096
ds_read_b64_tr_b16 a[18:19], v16 offset:6144
buffer_load_dwordx4 v[156:159], v1, s[16:19], s91 offen offset:3072 sc1
v_mfma_f32_32x32x16_bf16 v[240:255], a[28:31], v[124:127], v[240:255]
ds_read_b64_tr_b16 a[20:21], v17 offset:4096
ds_read_b64_tr_b16 a[22:23], v17 offset:6144
s_waitcnt vmcnt(7)
v_mfma_f32_32x32x16_bf16 v[224:239], a[32:35], v[128:131], v[224:239]
ds_read_b64_tr_b16 a[24:25], v16 offset:5120
ds_read_b64_tr_b16 a[26:27], v16 offset:7168
buffer_load_dwordx4 v[160:163], v1, s[16:19], s92 offen sc1
v_add_f32 v80, v80, v81
v_add_f32 v80, v80, v82
v_add_f32 v80, v80, v83
v_mfma_f32_32x32x16_bf16 v[240:255], a[36:39], v[128:131], v[240:255]
ds_read_b64_tr_b16 a[28:29], v17 offset:5120
ds_read_b64_tr_b16 a[30:31], v17 offset:7168
v_add_f32 v80, v80, v84
v_add_f32 v80, v80, v85
v_add_f32 v80, v80, v86
v_mov_b32 v81, v80
s_waitcnt vmcnt(7)
v_mfma_f32_32x32x16_bf16 v[224:239], a[40:43], v[132:135], v[224:239]
ds_read_b64_tr_b16 a[32:33], v16 offset:8192
ds_read_b64_tr_b16 a[34:35], v16 offset:10240
buffer_load_dwordx4 v[164:167], v1, s[16:19], s92 offen offset:1024 sc1
v_permlane32_swap_b32 v80, v81
v_mul_f32 v80, 0x3b124925, v80
v_mul_f32 v81, 0x3b124925, v81
v_fma_f32 v81, -v80, v80, v81
v_mfma_f32_32x32x16_bf16 v[240:255], a[44:47], v[132:135], v[240:255]
ds_read_b64_tr_b16 a[36:37], v17 offset:8192
ds_read_b64_tr_b16 a[38:39], v17 offset:10240
v_max_f32 v81, 0, v81
v_add_f32 v81, 0x3727c5ac, v81
v_rsq_f32 v20, v81
v_cvt_pk_bf16_f32 v44, -v80, -v80
s_waitcnt vmcnt(7)
v_mfma_f32_32x32x16_bf16 v[224:239], a[48:51], v[136:139], v[224:239]
ds_read_b64_tr_b16 a[40:41], v16 offset:9216
ds_read_b64_tr_b16 a[42:43], v16 offset:11264
buffer_load_dwordx4 v[168:171], v1, s[16:19], s92 offen offset:2048 sc1
v_mov_b32 v46, 0
v_lshlrev_b32 v48, 16, v44
v_add_f32 v48, v48, v80
v_mov_b32 v47, 0
v_mfma_f32_32x32x16_bf16 v[240:255], a[52:55], v[136:139], v[240:255]
ds_read_b64_tr_b16 a[44:45], v17 offset:9216
ds_read_b64_tr_b16 a[46:47], v17 offset:11264
v_cvt_pk_bf16_f32 v45, -v48, -v48
s_waitcnt vmcnt(7)
v_mfma_f32_32x32x16_bf16 v[224:239], a[56:59], v[140:143], v[224:239]
ds_read_b64_tr_b16 a[48:49], v16 offset:12288
ds_read_b64_tr_b16 a[50:51], v16 offset:14336
buffer_load_dwordx4 v[172:175], v1, s[16:19], s92 offen offset:3072 sc1
v_mfma_f32_32x32x16_bf16 v[240:255], a[60:63], v[140:143], v[240:255]
ds_read_b64_tr_b16 a[52:53], v17 offset:12288
ds_read_b64_tr_b16 a[54:55], v17 offset:14336
s_waitcnt vmcnt(7)
v_mfma_f32_32x32x16_bf16 v[224:239], a[64:67], v[144:147], v[224:239]
ds_read_b64_tr_b16 a[56:57], v16 offset:13312
ds_read_b64_tr_b16 a[58:59], v16 offset:15360
buffer_load_dwordx4 v[176:179], v1, s[16:19], s93 offen sc1
v_mfma_f32_32x32x16_bf16 v[240:255], a[68:71], v[144:147], v[240:255]
ds_read_b64_tr_b16 a[60:61], v17 offset:13312
ds_read_b64_tr_b16 a[62:63], v17 offset:15360
v_mfma_f32_32x32x16_bf16 v[224:239], v[56:59], v[44:47], v[224:239]
v_mfma_f32_32x32x16_bf16 v[240:255], v[60:63], v[44:47], v[240:255]
s_waitcnt vmcnt(7)
v_mfma_f32_32x32x16_bf16 v[224:239], a[72:75], v[148:151], v[224:239]
ds_read_b64_tr_b16 a[64:65], v16 offset:16384
ds_read_b64_tr_b16 a[66:67], v16 offset:18432
buffer_load_dwordx4 v[180:183], v1, s[16:19], s93 offen offset:1024 sc1
v_mfma_f32_32x32x16_bf16 v[240:255], a[76:79], v[148:151], v[240:255]
ds_read_b64_tr_b16 a[68:69], v17 offset:16384
ds_read_b64_tr_b16 a[70:71], v17 offset:18432
s_waitcnt vmcnt(7)
v_mfma_f32_32x32x16_bf16 v[224:239], a[80:83], v[152:155], v[224:239]
ds_read_b64_tr_b16 a[72:73], v16 offset:17408
ds_read_b64_tr_b16 a[74:75], v16 offset:19456
buffer_load_dwordx4 v[184:187], v1, s[16:19], s93 offen offset:2048 sc1
v_mfma_f32_32x32x16_bf16 v[240:255], a[84:87], v[152:155], v[240:255]
ds_read_b64_tr_b16 a[76:77], v17 offset:17408
ds_read_b64_tr_b16 a[78:79], v17 offset:19456
v_cndmask_b32_e64 v56, v19, 0, s[74:75]
v_mov_b32 v58, 0
v_mov_b32 v57, v56
v_mov_b32 v59, 0
s_waitcnt vmcnt(7)
v_mfma_f32_32x32x16_bf16 v[224:239], a[88:91], v[156:159], v[224:239]
ds_read_b64_tr_b16 a[80:81], v16 offset:20480
ds_read_b64_tr_b16 a[82:83], v16 offset:22528
buffer_load_dwordx4 v[188:191], v1, s[16:19], s93 offen offset:3072 sc1
v_mfma_f32_32x32x16_bf16 v[240:255], a[92:95], v[156:159], v[240:255]
ds_read_b64_tr_b16 a[84:85], v17 offset:20480
ds_read_b64_tr_b16 a[86:87], v17 offset:22528
v_cndmask_b32_e64 v60, 0, v19, s[74:75]
v_mov_b32 v62, 0
v_mov_b32 v61, v60
v_mov_b32 v63, 0
s_waitcnt vmcnt(7)
v_mfma_f32_32x32x16_bf16 v[224:239], a[96:99], v[160:163], v[224:239]
ds_read_b64_tr_b16 a[88:89], v16 offset:21504
ds_read_b64_tr_b16 a[90:91], v16 offset:23552
buffer_load_dwordx4 v[192:195], v1, s[16:19], s94 offen sc1
v_mfma_f32_32x32x16_bf16 v[240:255], a[100:103], v[160:163], v[240:255]
ds_read_b64_tr_b16 a[92:93], v17 offset:21504
ds_read_b64_tr_b16 a[94:95], v17 offset:23552
s_waitcnt vmcnt(7)
v_mfma_f32_32x32x16_bf16 v[224:239], a[104:107], v[164:167], v[224:239]
ds_read_b64_tr_b16 a[96:97], v16 offset:24576
ds_read_b64_tr_b16 a[98:99], v16 offset:26624
buffer_load_dwordx4 v[196:199], v1, s[16:19], s94 offen offset:1024 sc1
v_mfma_f32_32x32x16_bf16 v[240:255], a[108:111], v[164:167], v[240:255]
ds_read_b64_tr_b16 a[100:101], v17 offset:24576
ds_read_b64_tr_b16 a[102:103], v17 offset:26624
s_waitcnt vmcnt(7)
v_mfma_f32_32x32x16_bf16 v[224:239], a[112:115], v[168:171], v[224:239]
ds_read_b64_tr_b16 a[104:105], v16 offset:25600
ds_read_b64_tr_b16 a[106:107], v16 offset:27648
buffer_load_dwordx4 v[200:203], v1, s[16:19], s94 offen offset:2048 sc1
v_mfma_f32_32x32x16_bf16 v[240:255], a[116:119], v[168:171], v[240:255]
ds_read_b64_tr_b16 a[108:109], v17 offset:25600
ds_read_b64_tr_b16 a[110:111], v17 offset:27648
s_waitcnt vmcnt(7)
v_mfma_f32_32x32x16_bf16 v[224:239], a[120:123], v[172:175], v[224:239]
ds_read_b64_tr_b16 a[112:113], v16 offset:28672
ds_read_b64_tr_b16 a[114:115], v16 offset:30720
buffer_load_dwordx4 v[204:207], v1, s[16:19], s94 offen offset:3072 sc1
v_mfma_f32_32x32x16_bf16 v[240:255], a[124:127], v[172:175], v[240:255]
ds_read_b64_tr_b16 a[116:117], v17 offset:28672
ds_read_b64_tr_b16 a[118:119], v17 offset:30720
s_waitcnt vmcnt(7)
v_mfma_f32_32x32x16_bf16 v[224:239], a[128:131], v[176:179], v[224:239]
ds_read_b64_tr_b16 a[120:121], v16 offset:29696
ds_read_b64_tr_b16 a[122:123], v16 offset:31744
buffer_load_dwordx4 v[208:211], v1, s[16:19], s95 offen sc1
v_mfma_f32_32x32x16_bf16 v[240:255], a[132:135], v[176:179], v[240:255]
ds_read_b64_tr_b16 a[124:125], v17 offset:29696
ds_read_b64_tr_b16 a[126:127], v17 offset:31744
s_waitcnt vmcnt(7)
v_mfma_f32_32x32x16_bf16 v[224:239], a[136:139], v[180:183], v[224:239]
ds_read_b64_tr_b16 a[128:129], v16 offset:32768
ds_read_b64_tr_b16 a[130:131], v16 offset:34816
buffer_load_dwordx4 v[212:215], v1, s[16:19], s95 offen offset:1024 sc1
v_mfma_f32_32x32x16_bf16 v[240:255], a[140:143], v[180:183], v[240:255]
ds_read_b64_tr_b16 a[132:133], v17 offset:32768
ds_read_b64_tr_b16 a[134:135], v17 offset:34816
s_waitcnt vmcnt(7)
v_mfma_f32_32x32x16_bf16 v[224:239], a[144:147], v[184:187], v[224:239]
ds_read_b64_tr_b16 a[136:137], v16 offset:33792
ds_read_b64_tr_b16 a[138:139], v16 offset:35840
buffer_load_dwordx4 v[216:219], v1, s[16:19], s95 offen offset:2048 sc1
v_mfma_f32_32x32x16_bf16 v[240:255], a[148:151], v[184:187], v[240:255]
ds_read_b64_tr_b16 a[140:141], v17 offset:33792
ds_read_b64_tr_b16 a[142:143], v17 offset:35840
s_waitcnt vmcnt(7)
v_mfma_f32_32x32x16_bf16 v[224:239], a[152:155], v[188:191], v[224:239]
ds_read_b64_tr_b16 a[144:145], v16 offset:36864
ds_read_b64_tr_b16 a[146:147], v16 offset:38912
buffer_load_dwordx4 v[220:223], v1, s[16:19], s95 offen offset:3072 sc1
v_mfma_f32_32x32x16_bf16 v[240:255], a[156:159], v[188:191], v[240:255]
ds_read_b64_tr_b16 a[148:149], v17 offset:36864
ds_read_b64_tr_b16 a[150:151], v17 offset:38912
s_waitcnt vmcnt(7)
v_mfma_f32_32x32x16_bf16 v[224:239], a[160:163], v[192:195], v[224:239]
ds_read_b64_tr_b16 a[152:153], v16 offset:37888
ds_read_b64_tr_b16 a[154:155], v16 offset:39936
s_add_u32 m0, s57, 0
s_add_u32 s6, s49, 0
s_and_b32 s6, s6, 3
s_lshl_b32 s6, s6, 16
s_add_u32 s6, s6, s87
s_xor_b32 s7, s88, 0
s_add_u32 s6, s6, s7
buffer_load_dwordx4 v42, s[28:31], s6 offen lds
v_mfma_f32_32x32x16_bf16 v[240:255], a[164:167], v[192:195], v[240:255]
ds_read_b64_tr_b16 a[156:157], v17 offset:37888
ds_read_b64_tr_b16 a[158:159], v17 offset:39936
s_waitcnt vmcnt(7)
v_mfma_f32_32x32x16_bf16 v[224:239], a[168:171], v[196:199], v[224:239]
ds_read_b64_tr_b16 a[160:161], v16 offset:40960
ds_read_b64_tr_b16 a[162:163], v16 offset:43008
buffer_load_dwordx4 v4, s[28:31], s6 offen offset:1024 lds
v_mfma_f32_32x32x16_bf16 v[240:255], a[172:175], v[196:199], v[240:255]
ds_read_b64_tr_b16 a[164:165], v17 offset:40960
ds_read_b64_tr_b16 a[166:167], v17 offset:43008
s_waitcnt vmcnt(7)
v_mfma_f32_32x32x16_bf16 v[224:239], a[176:179], v[200:203], v[224:239]
ds_read_b64_tr_b16 a[168:169], v16 offset:41984
ds_read_b64_tr_b16 a[170:171], v16 offset:44032
s_add_u32 m0, s57, 2048
s_add_u32 s6, s6, 128
buffer_load_dwordx4 v42, s[28:31], s6 offen lds
v_mfma_f32_32x32x16_bf16 v[240:255], a[180:183], v[200:203], v[240:255]
ds_read_b64_tr_b16 a[172:173], v17 offset:41984
ds_read_b64_tr_b16 a[174:175], v17 offset:44032
s_waitcnt vmcnt(7)
v_mfma_f32_32x32x16_bf16 v[224:239], a[184:187], v[204:207], v[224:239]
ds_read_b64_tr_b16 a[176:177], v16 offset:45056
ds_read_b64_tr_b16 a[178:179], v16 offset:47104
buffer_load_dwordx4 v4, s[28:31], s6 offen offset:1024 lds
v_mfma_f32_32x32x16_bf16 v[240:255], a[188:191], v[204:207], v[240:255]
ds_read_b64_tr_b16 a[180:181], v17 offset:45056
ds_read_b64_tr_b16 a[182:183], v17 offset:47104
s_waitcnt vmcnt(7)
v_mfma_f32_32x32x16_bf16 v[224:239], a[192:195], v[208:211], v[224:239]
ds_read_b64_tr_b16 a[184:185], v16 offset:46080
ds_read_b64_tr_b16 a[186:187], v16 offset:48128
s_add_u32 m0, s57, 4096
s_add_u32 s6, s6, 128
buffer_load_dwordx4 v42, s[28:31], s6 offen lds
v_mfma_f32_32x32x16_bf16 v[240:255], a[196:199], v[208:211], v[240:255]
ds_read_b64_tr_b16 a[188:189], v17 offset:46080
ds_read_b64_tr_b16 a[190:191], v17 offset:48128
s_waitcnt vmcnt(7)
v_mfma_f32_32x32x16_bf16 v[224:239], a[200:203], v[212:215], v[224:239]
ds_read_b64_tr_b16 a[192:193], v16 offset:49152
ds_read_b64_tr_b16 a[194:195], v16 offset:51200
buffer_load_dwordx4 v4, s[28:31], s6 offen offset:1024 lds
v_mfma_f32_32x32x16_bf16 v[240:255], a[204:207], v[212:215], v[240:255]
ds_read_b64_tr_b16 a[196:197], v17 offset:49152
ds_read_b64_tr_b16 a[198:199], v17 offset:51200
s_waitcnt vmcnt(7)
v_mfma_f32_32x32x16_bf16 v[224:239], a[208:211], v[216:219], v[224:239]
ds_read_b64_tr_b16 a[200:201], v16 offset:50176
ds_read_b64_tr_b16 a[202:203], v16 offset:52224
s_add_u32 m0, s57, 6144
s_add_u32 s6, s6, 128
buffer_load_dwordx4 v42, s[28:31], s6 offen lds
v_mfma_f32_32x32x16_bf16 v[240:255], a[212:215], v[216:219], v[240:255]
ds_read_b64_tr_b16 a[204:205], v17 offset:50176
ds_read_b64_tr_b16 a[206:207], v17 offset:52224
s_waitcnt vmcnt(7)
v_mfma_f32_32x32x16_bf16 v[224:239], a[216:219], v[220:223], v[224:239]
ds_read_b64_tr_b16 a[208:209], v16 offset:53248
ds_read_b64_tr_b16 a[210:211], v16 offset:55296
buffer_load_dwordx4 v4, s[28:31], s6 offen offset:1024 lds
v_mfma_f32_32x32x16_bf16 v[240:255], a[220:223], v[220:223], v[240:255]
ds_read_b64_tr_b16 a[212:213], v17 offset:53248
ds_read_b64_tr_b16 a[214:215], v17 offset:55296
ds_read_b64_tr_b16 a[216:217], v16 offset:54272
ds_read_b64_tr_b16 a[218:219], v16 offset:56320
ds_read_b64_tr_b16 a[220:221], v17 offset:54272
ds_read_b64_tr_b16 a[222:223], v17 offset:56320
s_nop 1
v_pk_mul_f32 v[224:225], v[224:225], v[20:21] op_sel_hi:[1,0]
v_pk_mul_f32 v[226:227], v[226:227], v[20:21] op_sel_hi:[1,0]
v_pk_mul_f32 v[228:229], v[228:229], v[20:21] op_sel_hi:[1,0]
v_pk_mul_f32 v[230:231], v[230:231], v[20:21] op_sel_hi:[1,0]
v_pk_mul_f32 v[48:49], v[224:225], v[224:225]
v_pk_mul_f32 v[50:51], v[226:227], v[226:227]
v_pk_mul_f32 v[52:53], v[228:229], v[228:229]
v_pk_mul_f32 v[54:55], v[230:231], v[230:231]
v_pk_fma_f32 v[48:49], v[48:49], s[80:81], v[10:11] op_sel_hi:[1,1,0]
v_pk_fma_f32 v[50:51], v[50:51], s[80:81], v[10:11] op_sel_hi:[1,1,0]
v_pk_fma_f32 v[52:53], v[52:53], s[80:81], v[10:11] op_sel_hi:[1,1,0]
v_pk_fma_f32 v[54:55], v[54:55], s[80:81], v[10:11] op_sel_hi:[1,1,0]
v_pk_mul_f32 v[48:49], v[48:49], v[224:225]
v_pk_mul_f32 v[50:51], v[50:51], v[226:227]
v_pk_mul_f32 v[52:53], v[52:53], v[228:229]
v_pk_mul_f32 v[54:55], v[54:55], v[230:231]
v_exp_f32 v48, v48
v_exp_f32 v49, v49
v_exp_f32 v50, v50
v_exp_f32 v51, v51
v_exp_f32 v52, v52
v_exp_f32 v53, v53
v_exp_f32 v54, v54
v_exp_f32 v55, v55
v_pk_add_f32 v[48:49], v[48:49], 1.0 op_sel_hi:[1,0]
v_pk_add_f32 v[50:51], v[50:51], 1.0 op_sel_hi:[1,0]
v_pk_add_f32 v[52:53], v[52:53], 1.0 op_sel_hi:[1,0]
v_pk_add_f32 v[54:55], v[54:55], 1.0 op_sel_hi:[1,0]
v_rcp_f32 v48, v48
v_rcp_f32 v49, v49
v_rcp_f32 v50, v50
v_rcp_f32 v51, v51
v_rcp_f32 v52, v52
v_rcp_f32 v53, v53
v_rcp_f32 v54, v54
v_rcp_f32 v55, v55
v_pk_mul_f32 v[224:225], v[224:225], v[48:49]
v_pk_mul_f32 v[226:227], v[226:227], v[50:51]
v_pk_mul_f32 v[228:229], v[228:229], v[52:53]
v_pk_mul_f32 v[230:231], v[230:231], v[54:55]
v_pk_mul_f32 v[232:233], v[232:233], v[20:21] op_sel_hi:[1,0]
v_pk_mul_f32 v[234:235], v[234:235], v[20:21] op_sel_hi:[1,0]
v_pk_mul_f32 v[236:237], v[236:237], v[20:21] op_sel_hi:[1,0]
v_pk_mul_f32 v[238:239], v[238:239], v[20:21] op_sel_hi:[1,0]
v_pk_mul_f32 v[48:49], v[232:233], v[232:233]
v_pk_mul_f32 v[50:51], v[234:235], v[234:235]
v_pk_mul_f32 v[52:53], v[236:237], v[236:237]
v_pk_mul_f32 v[54:55], v[238:239], v[238:239]
v_pk_fma_f32 v[48:49], v[48:49], s[80:81], v[10:11] op_sel_hi:[1,1,0]
v_pk_fma_f32 v[50:51], v[50:51], s[80:81], v[10:11] op_sel_hi:[1,1,0]
v_pk_fma_f32 v[52:53], v[52:53], s[80:81], v[10:11] op_sel_hi:[1,1,0]
v_pk_fma_f32 v[54:55], v[54:55], s[80:81], v[10:11] op_sel_hi:[1,1,0]
v_pk_mul_f32 v[48:49], v[48:49], v[232:233]
s_add_u32 m0, s57, 8192
s_add_u32 s6, s49, 1
s_and_b32 s6, s6, 3
s_lshl_b32 s6, s6, 16
s_add_u32 s6, s6, s87
s_xor_b32 s7, s88, 0
s_add_u32 s6, s6, s7
buffer_load_dwordx4 v42, s[28:31], s6 offen lds
v_pk_mul_f32 v[50:51], v[50:51], v[234:235]
v_pk_mul_f32 v[52:53], v[52:53], v[236:237]
v_pk_mul_f32 v[54:55], v[54:55], v[238:239]
v_exp_f32 v48, v48
v_exp_f32 v49, v49
v_exp_f32 v50, v50
v_exp_f32 v51, v51
v_exp_f32 v52, v52
v_exp_f32 v53, v53
v_exp_f32 v54, v54
v_exp_f32 v55, v55
v_pk_add_f32 v[48:49], v[48:49], 1.0 op_sel_hi:[1,0]
v_pk_add_f32 v[50:51], v[50:51], 1.0 op_sel_hi:[1,0]
v_pk_add_f32 v[52:53], v[52:53], 1.0 op_sel_hi:[1,0]
v_pk_add_f32 v[54:55], v[54:55], 1.0 op_sel_hi:[1,0]
v_rcp_f32 v48, v48
v_rcp_f32 v49, v49
v_rcp_f32 v50, v50
v_rcp_f32 v51, v51
v_rcp_f32 v52, v52
v_rcp_f32 v53, v53
v_rcp_f32 v54, v54
v_rcp_f32 v55, v55
v_pk_mul_f32 v[232:233], v[232:233], v[48:49]
v_pk_mul_f32 v[234:235], v[234:235], v[50:51]
v_pk_mul_f32 v[236:237], v[236:237], v[52:53]
v_pk_mul_f32 v[238:239], v[238:239], v[54:55]
v_pk_mul_f32 v[240:241], v[240:241], v[20:21] op_sel_hi:[1,0]
v_pk_mul_f32 v[242:243], v[242:243], v[20:21] op_sel_hi:[1,0]
v_pk_mul_f32 v[244:245], v[244:245], v[20:21] op_sel_hi:[1,0]
v_pk_mul_f32 v[246:247], v[246:247], v[20:21] op_sel_hi:[1,0]
v_pk_mul_f32 v[48:49], v[240:241], v[240:241]
v_pk_mul_f32 v[50:51], v[242:243], v[242:243]
v_pk_mul_f32 v[52:53], v[244:245], v[244:245]
v_pk_mul_f32 v[54:55], v[246:247], v[246:247]
v_pk_fma_f32 v[48:49], v[48:49], s[80:81], v[10:11] op_sel_hi:[1,1,0]
v_pk_fma_f32 v[50:51], v[50:51], s[80:81], v[10:11] op_sel_hi:[1,1,0]
v_pk_fma_f32 v[52:53], v[52:53], s[80:81], v[10:11] op_sel_hi:[1,1,0]
v_pk_fma_f32 v[54:55], v[54:55], s[80:81], v[10:11] op_sel_hi:[1,1,0]
v_pk_mul_f32 v[48:49], v[48:49], v[240:241]
v_pk_mul_f32 v[50:51], v[50:51], v[242:243]
v_pk_mul_f32 v[52:53], v[52:53], v[244:245]
v_pk_mul_f32 v[54:55], v[54:55], v[246:247]
v_exp_f32 v48, v48
v_exp_f32 v49, v49
v_exp_f32 v50, v50
v_exp_f32 v51, v51
v_exp_f32 v52, v52
v_exp_f32 v53, v53
v_exp_f32 v54, v54
v_exp_f32 v55, v55
v_pk_add_f32 v[48:49], v[48:49], 1.0 op_sel_hi:[1,0]
v_pk_add_f32 v[50:51], v[50:51], 1.0 op_sel_hi:[1,0]
buffer_load_dwordx4 v4, s[28:31], s6 offen offset:1024 lds
v_pk_add_f32 v[52:53], v[52:53], 1.0 op_sel_hi:[1,0]
v_pk_add_f32 v[54:55], v[54:55], 1.0 op_sel_hi:[1,0]
v_rcp_f32 v48, v48
v_rcp_f32 v49, v49
v_rcp_f32 v50, v50
v_rcp_f32 v51, v51
v_rcp_f32 v52, v52
v_rcp_f32 v53, v53
v_rcp_f32 v54, v54
v_rcp_f32 v55, v55
v_pk_mul_f32 v[240:241], v[240:241], v[48:49]
v_pk_mul_f32 v[242:243], v[242:243], v[50:51]
v_pk_mul_f32 v[244:245], v[244:245], v[52:53]
v_pk_mul_f32 v[246:247], v[246:247], v[54:55]
v_pk_mul_f32 v[248:249], v[248:249], v[20:21] op_sel_hi:[1,0]
v_pk_mul_f32 v[250:251], v[250:251], v[20:21] op_sel_hi:[1,0]
v_pk_mul_f32 v[252:253], v[252:253], v[20:21] op_sel_hi:[1,0]
v_pk_mul_f32 v[254:255], v[254:255], v[20:21] op_sel_hi:[1,0]
v_pk_mul_f32 v[48:49], v[248:249], v[248:249]
v_pk_mul_f32 v[50:51], v[250:251], v[250:251]
v_pk_mul_f32 v[52:53], v[252:253], v[252:253]
v_pk_mul_f32 v[54:55], v[254:255], v[254:255]
v_pk_fma_f32 v[48:49], v[48:49], s[80:81], v[10:11] op_sel_hi:[1,1,0]
v_pk_fma_f32 v[50:51], v[50:51], s[80:81], v[10:11] op_sel_hi:[1,1,0]
v_pk_fma_f32 v[52:53], v[52:53], s[80:81], v[10:11] op_sel_hi:[1,1,0]
v_pk_fma_f32 v[54:55], v[54:55], s[80:81], v[10:11] op_sel_hi:[1,1,0]
v_pk_mul_f32 v[48:49], v[48:49], v[248:249]
v_pk_mul_f32 v[50:51], v[50:51], v[250:251]
v_pk_mul_f32 v[52:53], v[52:53], v[252:253]
v_pk_mul_f32 v[54:55], v[54:55], v[254:255]
v_exp_f32 v48, v48
v_exp_f32 v49, v49
v_exp_f32 v50, v50
v_exp_f32 v51, v51
v_exp_f32 v52, v52
v_exp_f32 v53, v53
v_exp_f32 v54, v54
v_exp_f32 v55, v55
v_pk_add_f32 v[48:49], v[48:49], 1.0 op_sel_hi:[1,0]
v_pk_add_f32 v[50:51], v[50:51], 1.0 op_sel_hi:[1,0]
v_pk_add_f32 v[52:53], v[52:53], 1.0 op_sel_hi:[1,0]
v_pk_add_f32 v[54:55], v[54:55], 1.0 op_sel_hi:[1,0]
v_rcp_f32 v48, v48
v_rcp_f32 v49, v49
v_rcp_f32 v50, v50
v_rcp_f32 v51, v51
v_rcp_f32 v52, v52
v_rcp_f32 v53, v53
v_rcp_f32 v54, v54
v_rcp_f32 v55, v55
v_pk_mul_f32 v[248:249], v[248:249], v[48:49]
v_pk_mul_f32 v[250:251], v[250:251], v[50:51]
v_pk_mul_f32 v[252:253], v[252:253], v[52:53]
v_pk_mul_f32 v[254:255], v[254:255], v[54:55]
v_pk_add_f32 v[48:49], v[224:225], v[226:227]
v_pk_mul_f32 v[50:51], v[224:225], v[224:225]
v_pk_fma_f32 v[50:51], v[226:227], v[226:227], v[50:51]
v_pk_add_f32 v[48:49], v[48:49], v[228:229]
v_pk_fma_f32 v[50:51], v[228:229], v[228:229], v[50:51]
v_pk_add_f32 v[48:49], v[48:49], v[230:231]
v_pk_fma_f32 v[50:51], v[230:231], v[230:231], v[50:51]
v_pk_add_f32 v[48:49], v[48:49], v[232:233]
v_pk_fma_f32 v[50:51], v[232:233], v[232:233], v[50:51]
v_pk_add_f32 v[48:49], v[48:49], v[234:235]
v_pk_fma_f32 v[50:51], v[234:235], v[234:235], v[50:51]
v_pk_add_f32 v[48:49], v[48:49], v[236:237]
v_pk_fma_f32 v[50:51], v[236:237], v[236:237], v[50:51]
v_pk_add_f32 v[48:49], v[48:49], v[238:239]
v_pk_fma_f32 v[50:51], v[238:239], v[238:239], v[50:51]
v_pk_add_f32 v[48:49], v[48:49], v[240:241]
v_pk_fma_f32 v[50:51], v[240:241], v[240:241], v[50:51]
v_pk_add_f32 v[48:49], v[48:49], v[242:243]
v_pk_fma_f32 v[50:51], v[242:243], v[242:243], v[50:51]
v_pk_add_f32 v[48:49], v[48:49], v[244:245]
v_pk_fma_f32 v[50:51], v[244:245], v[244:245], v[50:51]
v_pk_add_f32 v[48:49], v[48:49], v[246:247]
v_pk_fma_f32 v[50:51], v[246:247], v[246:247], v[50:51]
v_pk_add_f32 v[48:49], v[48:49], v[248:249]
v_pk_fma_f32 v[50:51], v[248:249], v[248:249], v[50:51]
v_pk_add_f32 v[48:49], v[48:49], v[250:251]
v_pk_fma_f32 v[50:51], v[250:251], v[250:251], v[50:51]
v_pk_add_f32 v[48:49], v[48:49], v[252:253]
v_pk_fma_f32 v[50:51], v[252:253], v[252:253], v[50:51]
v_pk_add_f32 v[48:49], v[48:49], v[254:255]
v_pk_fma_f32 v[50:51], v[254:255], v[254:255], v[50:51]
v_add_f32 v48, v48, v49
v_add_f32 v50, v50, v51
s_nop 1
v_permlane32_swap_b32 v48, v50
v_add_f32 v14, v48, v50
v_and_b32 v14, -2, v14
buffer_store_dword v14, v21, s[16:19], s68 offen sc1
s_waitcnt lgkmcnt(0)
s_barrier
v_mfma_f32_32x32x16_bf16 v[64:79], v[56:59], v[44:47], 0
v_mfma_f32_32x32x16_bf16 v[80:95], v[60:63], v[44:47], 0
v_mfma_f32_32x32x16_bf16 v[64:79], a[0:3], v[112:115], v[64:79]
s_add_u32 m0, s58, 57344
s_add_u32 s6, s49, 1
s_and_b32 s6, s6, 3
s_lshl_b32 s6, s6, 16
s_add_u32 s6, s6, s87
s_xor_b32 s7, s88, 128
s_add_u32 s6, s6, s7
buffer_load_dwordx4 v42, s[28:31], s6 offen lds
v_mfma_f32_32x32x16_bf16 v[80:95], a[4:7], v[112:115], v[80:95]
v_mfma_f32_32x32x16_bf16 v[64:79], a[8:11], v[116:119], v[64:79]
buffer_load_dwordx4 v4, s[28:31], s6 offen offset:1024 lds
v_mfma_f32_32x32x16_bf16 v[80:95], a[12:15], v[116:119], v[80:95]
v_mfma_f32_32x32x16_bf16 v[64:79], a[16:19], v[120:123], v[64:79]
s_add_u32 m0, s58, 59392
s_add_u32 s6, s6, 128
buffer_load_dwordx4 v42, s[28:31], s6 offen lds
v_mfma_f32_32x32x16_bf16 v[80:95], a[20:23], v[120:123], v[80:95]
v_mfma_f32_32x32x16_bf16 v[64:79], a[24:27], v[124:127], v[64:79]
buffer_load_dwordx4 v4, s[28:31], s6 offen offset:1024 lds
v_mfma_f32_32x32x16_bf16 v[80:95], a[28:31], v[124:127], v[80:95]
v_mfma_f32_32x32x16_bf16 v[64:79], a[32:35], v[128:131], v[64:79]
s_add_u32 m0, s58, 61440
s_add_u32 s6, s6, 128
buffer_load_dwordx4 v42, s[28:31], s6 offen lds
v_mfma_f32_32x32x16_bf16 v[80:95], a[36:39], v[128:131], v[80:95]
v_mfma_f32_32x32x16_bf16 v[64:79], a[40:43], v[132:135], v[64:79]
buffer_load_dwordx4 v4, s[28:31], s6 offen offset:1024 lds
v_mfma_f32_32x32x16_bf16 v[80:95], a[44:47], v[132:135], v[80:95]
v_mfma_f32_32x32x16_bf16 v[64:79], a[48:51], v[136:139], v[64:79]
s_add_u32 m0, s58, 63488
s_add_u32 s6, s49, 2
s_and_b32 s6, s6, 3
s_lshl_b32 s6, s6, 16
s_add_u32 s6, s6, s87
s_xor_b32 s7, s88, 0
s_add_u32 s6, s6, s7
buffer_load_dwordx4 v42, s[28:31], s6 offen lds
v_mfma_f32_32x32x16_bf16 v[80:95], a[52:55], v[136:139], v[80:95]
v_mfma_f32_32x32x16_bf16 v[64:79], a[56:59], v[140:143], v[64:79]
buffer_load_dwordx4 v4, s[28:31], s6 offen offset:1024 lds
v_mfma_f32_32x32x16_bf16 v[80:95], a[60:63], v[140:143], v[80:95]
v_mfma_f32_32x32x16_bf16 v[64:79], a[64:67], v[144:147], v[64:79]
s_add_u32 m0, s58, 65536
s_add_u32 s6, s6, 128
buffer_load_dwordx4 v42, s[28:31], s6 offen lds
v_mfma_f32_32x32x16_bf16 v[80:95], a[68:71], v[144:147], v[80:95]
v_mfma_f32_32x32x16_bf16 v[64:79], a[72:75], v[148:151], v[64:79]
buffer_load_dwordx4 v4, s[28:31], s6 offen offset:1024 lds
v_mfma_f32_32x32x16_bf16 v[80:95], a[76:79], v[148:151], v[80:95]
v_mfma_f32_32x32x16_bf16 v[64:79], a[80:83], v[152:155], v[64:79]
s_add_u32 m0, s58, 67584
s_add_u32 s6, s6, 128
buffer_load_dwordx4 v42, s[28:31], s6 offen lds
v_mfma_f32_32x32x16_bf16 v[80:95], a[84:87], v[152:155], v[80:95]
v_mfma_f32_32x32x16_bf16 v[64:79], a[88:91], v[156:159], v[64:79]
buffer_load_dwordx4 v4, s[28:31], s6 offen offset:1024 lds
v_mfma_f32_32x32x16_bf16 v[80:95], a[92:95], v[156:159], v[80:95]
v_mfma_f32_32x32x16_bf16 v[64:79], a[96:99], v[160:163], v[64:79]
s_add_u32 m0, s58, 69632
s_add_u32 s6, s6, 128
buffer_load_dwordx4 v42, s[28:31], s6 offen lds
s_mul_i32 s67, s55, 56
s_lshl1_add_u32 s67, s51, s67
v_mfma_f32_32x32x16_bf16 v[80:95], a[100:103], v[160:163], v[80:95]
s_lshl_b32 s67, s67, 10
s_add_u32 s59, s67, 12582912
v_mfma_f32_32x32x16_bf16 v[64:79], a[104:107], v[164:167], v[64:79]
buffer_load_dwordx4 v4, s[28:31], s6 offen offset:1024 lds
v_bfe_u32 v18, v0, 4, 1
v_mul_u32_u24 v18, 28672, v18
v_mfma_f32_32x32x16_bf16 v[80:95], a[108:111], v[164:167], v[80:95]
v_lshrrev_b32 v19, 5, v0
v_lshl_add_u32 v18, v19, 8, v18
v_mfma_f32_32x32x16_bf16 v[64:79], a[112:115], v[168:171], v[64:79]
s_add_u32 m0, s58, 0
s_add_u32 s6, s49, 3
s_and_b32 s6, s6, 3
s_lshl_b32 s6, s6, 16
s_add_u32 s6, s6, s87
s_xor_b32 s7, s88, 0
s_add_u32 s6, s6, s7
buffer_load_dwordx4 v42, s[28:31], s6 offen lds
v_and_b32 v19, 15, v0
v_lshl_add_u32 v18, v19, 4, v18
v_mfma_f32_32x32x16_bf16 v[80:95], a[116:119], v[168:171], v[80:95]
v_and_b32 v12, 9, v0
v_bfe_u32 v13, v0, 1, 1
v_mfma_f32_32x32x16_bf16 v[64:79], a[120:123], v[172:175], v[64:79]
buffer_load_dwordx4 v4, s[28:31], s6 offen offset:1024 lds
v_lshl_or_b32 v12, v13, 2, v12
v_bfe_u32 v13, v0, 2, 1
v_mfma_f32_32x32x16_bf16 v[80:95], a[124:127], v[172:175], v[80:95]
v_lshl_or_b32 v12, v13, 1, v12
v_lshrrev_b32 v13, 5, v0
v_mfma_f32_32x32x16_bf16 v[64:79], a[128:131], v[176:179], v[64:79]
s_add_u32 m0, s58, 2048
s_add_u32 s6, s6, 128
buffer_load_dwordx4 v42, s[28:31], s6 offen lds
v_xor_b32 v12, v12, v13
v_and_b32 v13, 31, v0
v_mfma_f32_32x32x16_bf16 v[80:95], a[132:135], v[176:179], v[80:95]
v_lshl_add_u32 v13, v13, 7, s57
v_lshl_add_u32 v128, v12, 3, v13
v_mfma_f32_32x32x16_bf16 v[64:79], a[136:139], v[180:183], v[64:79]
buffer_load_dwordx4 v4, s[28:31], s6 offen offset:1024 lds
v_xor_b32 v129, 16, v128
v_xor_b32 v130, 32, v128
v_mfma_f32_32x32x16_bf16 v[80:95], a[140:143], v[180:183], v[80:95]
v_xor_b32 v131, 48, v128
v_xor_b32 v132, 64, v128
v_mfma_f32_32x32x16_bf16 v[64:79], a[144:147], v[184:187], v[64:79]
s_add_u32 m0, s58, 4096
s_add_u32 s6, s6, 128
buffer_load_dwordx4 v42, s[28:31], s6 offen lds
v_xor_b32 v133, 80, v128
v_xor_b32 v134, 96, v128
v_mfma_f32_32x32x16_bf16 v[80:95], a[148:151], v[184:187], v[80:95]
v_xor_b32 v135, 112, v128
v_bfe_u32 v12, v0, 2, 1
v_mfma_f32_32x32x16_bf16 v[64:79], a[152:155], v[188:191], v[64:79]
buffer_load_dwordx4 v4, s[28:31], s6 offen offset:1024 lds
v_bfe_u32 v13, v0, 3, 2
v_lshl_or_b32 v12, v13, 2, v12
v_mfma_f32_32x32x16_bf16 v[80:95], a[156:159], v[188:191], v[80:95]
v_and_b32 v13, 3, v0
v_xor_b32 v12, v12, v13
v_mfma_f32_32x32x16_bf16 v[64:79], a[160:163], v[192:195], v[64:79]
buffer_load_dword v48, v21, s[16:19], s56 offen sc1
buffer_load_dword v49, v21, s[16:19], s56 offen offset:256 sc1
buffer_load_dword v50, v21, s[16:19], s56 offen offset:512 sc1
buffer_load_dword v51, v21, s[16:19], s56 offen offset:768 sc1
buffer_load_dword v52, v21, s[16:19], s56 offen offset:1024 sc1
buffer_load_dword v53, v21, s[16:19], s56 offen offset:1280 sc1
buffer_load_dword v54, v21, s[16:19], s56 offen offset:1536 sc1
buffer_load_dword v55, v21, s[16:19], s56 offen offset:1792 sc1
buffer_load_dword v56, v21, s[16:19], s56 offen offset:2048 sc1
buffer_load_dword v57, v21, s[16:19], s56 offen offset:2304 sc1
buffer_load_dword v58, v21, s[16:19], s56 offen offset:2560 sc1
buffer_load_dword v59, v21, s[16:19], s56 offen offset:2816 sc1
buffer_load_dword v60, v21, s[16:19], s56 offen offset:3072 sc1
buffer_load_dword v61, v21, s[16:19], s56 offen offset:3328 sc1
s_add_u32 s7, s57, 0
v_add_u32 v6, s7, v5
v_xor_b32 v7, 64, v6
s_waitcnt vmcnt(43)
ds_read_b128 a[0:3], v6 offset:0
v_lshrrev_b32 v13, 4, v0
v_bfe_u32 v15, v0, 2, 2
v_mfma_f32_32x32x16_bf16 v[80:95], a[164:167], v[192:195], v[80:95]
ds_read_b128 a[4:7], v7 offset:0
v_lshl_add_u32 v13, v13, 3, v15
v_lshl_add_u32 v13, v13, 7, s57
v_mfma_f32_32x32x16_bf16 v[64:79], a[168:171], v[196:199], v[64:79]
s_add_u32 m0, s58, 6144
s_add_u32 s6, s6, 128
buffer_load_dwordx4 v42, s[28:31], s6 offen lds
s_waitcnt vmcnt(42)
ds_read_b128 a[8:11], v6 offset:2048
v_lshl_add_u32 v136, v12, 3, v13
v_xor_b32 v137, 16, v136
v_mfma_f32_32x32x16_bf16 v[80:95], a[172:175], v[196:199], v[80:95]
ds_read_b128 a[12:15], v7 offset:2048
v_xor_b32 v138, 32, v136
v_xor_b32 v139, 48, v136
v_mfma_f32_32x32x16_bf16 v[64:79], a[176:179], v[200:203], v[64:79]
buffer_load_dwordx4 v4, s[28:31], s6 offen offset:1024 lds
s_waitcnt vmcnt(41)
ds_read_b128 a[16:19], v6 offset:4096
v_xor_b32 v140, 64, v136
v_xor_b32 v141, 80, v136
v_mfma_f32_32x32x16_bf16 v[80:95], a[180:183], v[200:203], v[80:95]
ds_read_b128 a[20:23], v7 offset:4096
v_xor_b32 v142, 96, v136
v_xor_b32 v143, 112, v136
v_mfma_f32_32x32x16_bf16 v[64:79], a[184:187], v[204:207], v[64:79]
s_add_u32 m0, s58, 8192
s_add_u32 s6, s49, 0
s_and_b32 s6, s6, 3
s_lshl_b32 s6, s6, 16
s_add_u32 s6, s6, s87
s_xor_b32 s7, s88, 512
s_add_u32 s6, s6, s7
buffer_load_dwordx4 v42, s[28:31], s6 offen lds
s_waitcnt vmcnt(40)
ds_read_b128 a[24:27], v6 offset:6144
s_lshl_b32 s71, s51, 8
s_add_u32 s71, s71, s55
v_mfma_f32_32x32x16_bf16 v[80:95], a[188:191], v[204:207], v[80:95]
ds_read_b128 a[28:31], v7 offset:6144
s_lshl_b32 s71, s71, 10
s_add_u32 s71, s71, 8388608
v_mfma_f32_32x32x16_bf16 v[64:79], a[192:195], v[208:211], v[64:79]
buffer_load_dwordx4 v4, s[28:31], s6 offen offset:1024 lds
s_waitcnt vmcnt(39)
ds_read_b128 a[64:67], v6 offset:8192
v_mfma_f32_32x32x16_bf16 v[80:95], a[196:199], v[208:211], v[80:95]
ds_read_b128 a[68:71], v7 offset:8192
v_mfma_f32_32x32x16_bf16 v[64:79], a[200:203], v[212:215], v[64:79]
s_add_u32 m0, s58, 10240
s_add_u32 s6, s6, 128
buffer_load_dwordx4 v42, s[28:31], s6 offen lds
v_mfma_f32_32x32x16_bf16 v[80:95], a[204:207], v[212:215], v[80:95]
v_mfma_f32_32x32x16_bf16 v[64:79], a[208:211], v[216:219], v[64:79]
buffer_load_dwordx4 v4, s[28:31], s6 offen offset:1024 lds
v_mfma_f32_32x32x16_bf16 v[80:95], a[212:215], v[216:219], v[80:95]
v_mfma_f32_32x32x16_bf16 v[64:79], a[216:219], v[220:223], v[64:79]
s_add_u32 m0, s58, 12288
s_add_u32 s6, s6, 128
buffer_load_dwordx4 v42, s[28:31], s6 offen lds
v_mfma_f32_32x32x16_bf16 v[80:95], a[220:223], v[220:223], v[80:95]
s_waitcnt vmcnt(7)
v_max3_u32 v62, v48, v49, v50
v_max3_u32 v62, v62, v51, v52
v_max3_u32 v62, v62, v53, v54
v_max3_u32 v62, v62, v55, v56
v_max3_u32 v62, v62, v57, v58
v_max3_u32 v62, v62, v59, v60
v_max_u32 v62, v62, v61
v_cmp_eq_u32_e32 vcc, -1, v62
s_nop 4
s_cbranch_vccz .Lk_stdone_13
s_mov_b32 s66, 32768
.Lk_st_12:
buffer_load_dword v48, v21, s[16:19], s56 offen sc1
buffer_load_dword v49, v21, s[16:19], s56 offen offset:256 sc1
buffer_load_dword v50, v21, s[16:19], s56 offen offset:512 sc1
buffer_load_dword v51, v21, s[16:19], s56 offen offset:768 sc1
buffer_load_dword v52, v21, s[16:19], s56 offen offset:1024 sc1
buffer_load_dword v53, v21, s[16:19], s56 offen offset:1280 sc1
buffer_load_dword v54, v21, s[16:19], s56 offen offset:1536 sc1
buffer_load_dword v55, v21, s[16:19], s56 offen offset:1792 sc1
buffer_load_dword v56, v21, s[16:19], s56 offen offset:2048 sc1
buffer_load_dword v57, v21, s[16:19], s56 offen offset:2304 sc1
buffer_load_dword v58, v21, s[16:19], s56 offen offset:2560 sc1
buffer_load_dword v59, v21, s[16:19], s56 offen offset:2816 sc1
buffer_load_dword v60, v21, s[16:19], s56 offen offset:3072 sc1
buffer_load_dword v61, v21, s[16:19], s56 offen offset:3328 sc1
s_waitcnt vmcnt(0)
v_max3_u32 v62, v48, v49, v50
v_max3_u32 v62, v62, v51, v52
v_max3_u32 v62, v62, v53, v54
v_max3_u32 v62, v62, v55, v56
v_max3_u32 v62, v62, v57, v58
v_max3_u32 v62, v62, v59, v60
v_max_u32 v62, v62, v61
v_cmp_eq_u32_e32 vcc, -1, v62
s_nop 4
s_cbranch_vccz .Lk_stdone_13
s_sub_u32 s66, s66, 1
s_cbranch_scc0 .Lk_st_12
.Lk_stdone_13:
v_pk_add_f32 v[48:49], v[48:49], v[50:51]
v_pk_add_f32 v[52:53], v[52:53], v[54:55]
v_pk_add_f32 v[56:57], v[56:57], v[58:59]
v_pk_add_f32 v[48:49], v[48:49], v[52:53]
v_pk_add_f32 v[56:57], v[56:57], v[60:61]
v_pk_add_f32 v[48:49], v[48:49], v[56:57]
v_add_f32 v48, v48, v49
v_mov_b32 v49, v48
s_nop 1
v_permlane32_swap_b32 v48, v49
v_mul_f32 v48, 0x3a924925, v48
v_mul_f32 v49, 0x3a924925, v49
v_fma_f32 v49, -v48, v48, v49
v_max_f32 v49, 0, v49
v_add_f32 v49, 0x3727c5ac, v49
v_rsq_f32 v50, v49
s_nop 0
v_mul_f32 v52, v48, v50
v_pk_fma_f32 v[224:225], v[224:225], v[50:51], v[52:53] op_sel_hi:[1,0,0] neg_lo:[0,0,1] neg_hi:[0,0,1]
v_pk_fma_f32 v[226:227], v[226:227], v[50:51], v[52:53] op_sel_hi:[1,0,0] neg_lo:[0,0,1] neg_hi:[0,0,1]
v_cvt_pk_bf16_f32 v224, v224, v225
v_cvt_pk_bf16_f32 v225, v226, v227
ds_write_b64 v128, v[224:225] offset:0
v_pk_fma_f32 v[228:229], v[228:229], v[50:51], v[52:53] op_sel_hi:[1,0,0] neg_lo:[0,0,1] neg_hi:[0,0,1]
v_pk_fma_f32 v[230:231], v[230:231], v[50:51], v[52:53] op_sel_hi:[1,0,0] neg_lo:[0,0,1] neg_hi:[0,0,1]
v_cvt_pk_bf16_f32 v226, v228, v229
v_cvt_pk_bf16_f32 v227, v230, v231
ds_write_b64 v129, v[226:227] offset:0
ds_read_b64_tr_b16 v[112:113], v136 offset:0
ds_read_b64_tr_b16 v[114:115], v137 offset:512
v_pk_fma_f32 v[232:233], v[232:233], v[50:51], v[52:53] op_sel_hi:[1,0,0] neg_lo:[0,0,1] neg_hi:[0,0,1]
v_pk_fma_f32 v[234:235], v[234:235], v[50:51], v[52:53] op_sel_hi:[1,0,0] neg_lo:[0,0,1] neg_hi:[0,0,1]
v_cvt_pk_bf16_f32 v228, v232, v233
v_cvt_pk_bf16_f32 v229, v234, v235
ds_write_b64 v130, v[228:229] offset:0
v_pk_fma_f32 v[236:237], v[236:237], v[50:51], v[52:53] op_sel_hi:[1,0,0] neg_lo:[0,0,1] neg_hi:[0,0,1]
v_pk_fma_f32 v[238:239], v[238:239], v[50:51], v[52:53] op_sel_hi:[1,0,0] neg_lo:[0,0,1] neg_hi:[0,0,1]
v_cvt_pk_bf16_f32 v230, v236, v237
v_cvt_pk_bf16_f32 v231, v238, v239
ds_write_b64 v131, v[230:231] offset:0
ds_read_b64_tr_b16 v[116:117], v138 offset:0
ds_read_b64_tr_b16 v[118:119], v139 offset:512
s_waitcnt lgkmcnt(4)
buffer_store_dwordx4 v[112:115], v1, s[16:19], s71 offen sc1
s_add_u32 s71, s71, 65536
v_pk_fma_f32 v[240:241], v[240:241], v[50:51], v[52:53] op_sel_hi:[1,0,0] neg_lo:[0,0,1] neg_hi:[0,0,1]
v_pk_fma_f32 v[242:243], v[242:243], v[50:51], v[52:53] op_sel_hi:[1,0,0] neg_lo:[0,0,1] neg_hi:[0,0,1]
v_cvt_pk_bf16_f32 v232, v240, v241
v_cvt_pk_bf16_f32 v233, v242, v243
ds_write_b64 v132, v[232:233] offset:0
v_pk_fma_f32 v[244:245], v[244:245], v[50:51], v[52:53] op_sel_hi:[1,0,0] neg_lo:[0,0,1] neg_hi:[0,0,1]
v_pk_fma_f32 v[246:247], v[246:247], v[50:51], v[52:53] op_sel_hi:[1,0,0] neg_lo:[0,0,1] neg_hi:[0,0,1]
v_cvt_pk_bf16_f32 v234, v244, v245
v_cvt_pk_bf16_f32 v235, v246, v247
ds_write_b64 v133, v[234:235] offset:0
ds_read_b64_tr_b16 v[120:121], v140 offset:0
ds_read_b64_tr_b16 v[122:123], v141 offset:512
s_waitcnt lgkmcnt(4)
buffer_store_dwordx4 v[116:119], v1, s[16:19], s71 offen sc1
s_add_u32 s71, s71, 65536
v_pk_fma_f32 v[248:249], v[248:249], v[50:51], v[52:53] op_sel_hi:[1,0,0] neg_lo:[0,0,1] neg_hi:[0,0,1]
v_pk_fma_f32 v[250:251], v[250:251], v[50:51], v[52:53] op_sel_hi:[1,0,0] neg_lo:[0,0,1] neg_hi:[0,0,1]
v_cvt_pk_bf16_f32 v236, v248, v249
v_cvt_pk_bf16_f32 v237, v250, v251
ds_write_b64 v134, v[236:237] offset:0
v_pk_fma_f32 v[252:253], v[252:253], v[50:51], v[52:53] op_sel_hi:[1,0,0] neg_lo:[0,0,1] neg_hi:[0,0,1]
v_pk_fma_f32 v[254:255], v[254:255], v[50:51], v[52:53] op_sel_hi:[1,0,0] neg_lo:[0,0,1] neg_hi:[0,0,1]
v_cvt_pk_bf16_f32 v238, v252, v253
v_cvt_pk_bf16_f32 v239, v254, v255
ds_write_b64 v135, v[238:239] offset:0
ds_read_b64_tr_b16 v[124:125], v142 offset:0
ds_read_b64_tr_b16 v[126:127], v143 offset:512
s_waitcnt lgkmcnt(4)
buffer_store_dwordx4 v[120:123], v1, s[16:19], s71 offen sc1
s_add_u32 s71, s71, 65536
s_waitcnt lgkmcnt(0)
buffer_store_dwordx4 v[124:127], v1, s[16:19], s71 offen sc1
s_lshl_b32 s68, s51, 2
s_mul_i32 s69, s68, 37
s_lshr_b32 s69, s69, 8
s_add_u32 s68, s68, 3
s_mul_i32 s70, s68, 37
s_lshr_b32 s70, s70, 8
s_cmp_eq_u32 s69, s70
s_cselect_b32 s76, 1, 3
s_mul_i32 s76, s76, 0x55
v_and_b32 v16, 1, v0
v_add_u32 v16, s69, v16
v_lshrrev_b32 v17, 1, v0
v_lshl_add_u32 v17, v17, 3, v16
v_lshl_add_u32 v17, v17, 4, s50
v_lshlrev_b32 v17, 7, v17
v_add_u32 v17, 1114112, v17
s_and_b32 s78, s76, 3
s_lshl_b32 s68, s50, 3
v_add_u32 v16, s68, v16
v_lshlrev_b32 v16, 7, v16
v_add_u32 v16, 1064960, v16
s_and_b32 s68, s48, 7
s_lshr_b32 s69, s48, 6
s_lshl2_add_u32 s68, s68, s69
s_lshl_b32 s68, s68, 7
s_add_u32 s68, s68, 1050624
v_writelane_b32 v17, s68, 8
s_bfe_u32 s68, s48, 0x30003
s_cmp_eq_u32 s68, 6
s_cselect_b32 s68, 0x100, 0
s_or_b32 s76, s76, s68
v_pk_mul_f32 v[64:65], v[64:65], v[20:21] op_sel_hi:[1,0]
v_pk_mul_f32 v[66:67], v[66:67], v[20:21] op_sel_hi:[1,0]
v_pk_mul_f32 v[68:69], v[68:69], v[20:21] op_sel_hi:[1,0]
buffer_load_dwordx4 v4, s[28:31], s6 offen offset:1024 lds
v_pk_mul_f32 v[70:71], v[70:71], v[20:21] op_sel_hi:[1,0]
v_pk_mul_f32 v[48:49], v[64:65], v[64:65]
v_pk_mul_f32 v[50:51], v[66:67], v[66:67]
s_add_u32 s7, s58, 57344
v_add_u32 v6, s7, v5
v_xor_b32 v7, 64, v6
ds_read_b128 a[72:75], v6 offset:0
v_pk_mul_f32 v[52:53], v[68:69], v[68:69]
v_pk_mul_f32 v[54:55], v[70:71], v[70:71]
v_pk_fma_f32 v[48:49], v[48:49], s[80:81], v[10:11] op_sel_hi:[1,1,0]
ds_read_b128 a[76:79], v7 offset:0
v_pk_fma_f32 v[50:51], v[50:51], s[80:81], v[10:11] op_sel_hi:[1,1,0]
v_pk_fma_f32 v[52:53], v[52:53], s[80:81], v[10:11] op_sel_hi:[1,1,0]
v_pk_fma_f32 v[54:55], v[54:55], s[80:81], v[10:11] op_sel_hi:[1,1,0]
s_add_u32 m0, s57, 4096
s_add_u32 s6, s49, 0
s_and_b32 s6, s6, 3
s_lshl_b32 s6, s6, 16
s_add_u32 s6, s6, s87
s_xor_b32 s7, s88, 896
s_add_u32 s6, s6, s7
buffer_load_dwordx4 v42, s[28:31], s6 offen lds
v_pk_mul_f32 v[48:49], v[48:49], v[64:65]
v_pk_mul_f32 v[50:51], v[50:51], v[66:67]
v_pk_mul_f32 v[52:53], v[52:53], v[68:69]
ds_read_b128 a[80:83], v6 offset:2048
v_pk_mul_f32 v[54:55], v[54:55], v[70:71]
v_exp_f32 v48, v48
v_exp_f32 v49, v49
buffer_load_dwordx4 v4, s[28:31], s6 offen offset:1024 lds
v_exp_f32 v50, v50
v_exp_f32 v51, v51
v_exp_f32 v52, v52
ds_read_b128 a[84:87], v7 offset:2048
v_exp_f32 v53, v53
v_exp_f32 v54, v54
v_exp_f32 v55, v55
s_add_u32 m0, s57, 6144
s_add_u32 s6, s49, 1
s_and_b32 s6, s6, 3
s_lshl_b32 s6, s6, 16
s_add_u32 s6, s6, s87
s_xor_b32 s7, s88, 512
s_add_u32 s6, s6, s7
buffer_load_dwordx4 v42, s[28:31], s6 offen lds
v_pk_add_f32 v[48:49], v[48:49], 1.0 op_sel_hi:[1,0]
v_pk_add_f32 v[50:51], v[50:51], 1.0 op_sel_hi:[1,0]
v_pk_add_f32 v[52:53], v[52:53], 1.0 op_sel_hi:[1,0]
ds_read_b128 a[88:91], v6 offset:4096
v_pk_add_f32 v[54:55], v[54:55], 1.0 op_sel_hi:[1,0]
v_rcp_f32 v48, v48
v_rcp_f32 v49, v49
ds_read_b128 a[92:95], v7 offset:4096
v_rcp_f32 v50, v50
v_rcp_f32 v51, v51
v_rcp_f32 v52, v52
buffer_load_dwordx4 v4, s[28:31], s6 offen offset:1024 lds
v_rcp_f32 v53, v53
v_rcp_f32 v54, v54
v_rcp_f32 v55, v55
ds_read_b128 a[128:131], v6 offset:6144
v_pk_mul_f32 v[64:65], v[64:65], v[48:49]
v_pk_mul_f32 v[66:67], v[66:67], v[50:51]
v_pk_mul_f32 v[68:69], v[68:69], v[52:53]
s_add_u32 m0, s57, 8192
s_add_u32 s6, s6, 128
buffer_load_dwordx4 v42, s[28:31], s6 offen lds
v_pk_mul_f32 v[70:71], v[70:71], v[54:55]
s_waitcnt vmcnt(6)
s_barrier
s_and_b32 exec_lo, s76, s3
s_mov_b32 exec_hi, 0
buffer_atomic_add v3, v17, s[16:19], 0 offen
s_mov_b64 exec, -1
v_pk_mul_f32 v[72:73], v[72:73], v[20:21] op_sel_hi:[1,0]
v_pk_mul_f32 v[74:75], v[74:75], v[20:21] op_sel_hi:[1,0]
v_pk_mul_f32 v[76:77], v[76:77], v[20:21] op_sel_hi:[1,0]
ds_read_b128 a[132:135], v7 offset:6144
v_pk_mul_f32 v[78:79], v[78:79], v[20:21] op_sel_hi:[1,0]
v_pk_mul_f32 v[48:49], v[72:73], v[72:73]
v_pk_mul_f32 v[50:51], v[74:75], v[74:75]
buffer_load_dwordx4 v4, s[28:31], s6 offen offset:1024 lds
v_pk_mul_f32 v[52:53], v[76:77], v[76:77]
v_pk_mul_f32 v[54:55], v[78:79], v[78:79]
v_pk_fma_f32 v[48:49], v[48:49], s[80:81], v[10:11] op_sel_hi:[1,1,0]
ds_read_b128 a[136:139], v6 offset:8192
v_pk_fma_f32 v[50:51], v[50:51], s[80:81], v[10:11] op_sel_hi:[1,1,0]
v_pk_fma_f32 v[52:53], v[52:53], s[80:81], v[10:11] op_sel_hi:[1,1,0]
v_pk_fma_f32 v[54:55], v[54:55], s[80:81], v[10:11] op_sel_hi:[1,1,0]
ds_read_b128 a[140:143], v7 offset:8192
v_pk_mul_f32 v[48:49], v[48:49], v[72:73]
v_pk_mul_f32 v[50:51], v[50:51], v[74:75]
v_pk_mul_f32 v[52:53], v[52:53], v[76:77]
s_waitcnt lgkmcnt(8)
s_add_u32 m0, s58, 57344
s_add_u32 s6, s6, 128
buffer_load_dwordx4 v42, s[28:31], s6 offen lds
v_pk_mul_f32 v[54:55], v[54:55], v[78:79]
v_exp_f32 v48, v48
v_exp_f32 v49, v49
ds_read_b128 a[144:147], v6 offset:10240
v_exp_f32 v50, v50
v_exp_f32 v51, v51
v_exp_f32 v52, v52
buffer_load_dwordx4 v4, s[28:31], s6 offen offset:1024 lds
v_exp_f32 v53, v53
v_exp_f32 v54, v54
v_exp_f32 v55, v55
ds_read_b128 a[148:151], v7 offset:10240
v_pk_add_f32 v[48:49], v[48:49], 1.0 op_sel_hi:[1,0]
v_pk_add_f32 v[50:51], v[50:51], 1.0 op_sel_hi:[1,0]
v_pk_add_f32 v[52:53], v[52:53], 1.0 op_sel_hi:[1,0]
s_waitcnt lgkmcnt(8)
s_add_u32 m0, s58, 59392
s_add_u32 s6, s6, 128
buffer_load_dwordx4 v42, s[28:31], s6 offen lds
v_pk_add_f32 v[54:55], v[54:55], 1.0 op_sel_hi:[1,0]
v_rcp_f32 v48, v48
v_rcp_f32 v49, v49
ds_read_b128 a[152:155], v6 offset:12288
v_rcp_f32 v50, v50
v_rcp_f32 v51, v51
v_rcp_f32 v52, v52
ds_read_b128 a[156:159], v7 offset:12288
v_rcp_f32 v53, v53
v_rcp_f32 v54, v54
v_rcp_f32 v55, v55
buffer_load_dwordx4 v4, s[28:31], s6 offen offset:1024 lds
v_pk_mul_f32 v[72:73], v[72:73], v[48:49]
v_pk_mul_f32 v[74:75], v[74:75], v[50:51]
v_pk_mul_f32 v[76:77], v[76:77], v[52:53]
s_add_u32 s7, s58, 0
v_add_u32 v6, s7, v5
v_xor_b32 v7, 64, v6
ds_read_b128 a[192:195], v6 offset:0
v_pk_mul_f32 v[78:79], v[78:79], v[54:55]
v_cvt_pk_bf16_f32 v144, v64, v65
v_cvt_pk_bf16_f32 v145, v66, v67
v_cvt_pk_bf16_f32 v148, v68, v69
v_cvt_pk_bf16_f32 v149, v70, v71
v_cvt_pk_bf16_f32 v146, v72, v73
v_cvt_pk_bf16_f32 v147, v74, v75
v_cvt_pk_bf16_f32 v150, v76, v77
v_cvt_pk_bf16_f32 v151, v78, v79
buffer_store_dwordx4 v[144:147], v18, s[16:19], s59 offen sc1
buffer_store_dwordx4 v[148:151], v18, s[16:19], s59 offen offset:512 sc1
v_pk_mul_f32 v[80:81], v[80:81], v[20:21] op_sel_hi:[1,0]
v_pk_mul_f32 v[82:83], v[82:83], v[20:21] op_sel_hi:[1,0]
v_pk_mul_f32 v[84:85], v[84:85], v[20:21] op_sel_hi:[1,0]
s_waitcnt lgkmcnt(9)
s_add_u32 m0, s58, 61440
s_add_u32 s6, s49, 2
s_and_b32 s6, s6, 3
s_lshl_b32 s6, s6, 16
s_add_u32 s6, s6, s87
s_xor_b32 s7, s88, 512
s_add_u32 s6, s6, s7
buffer_load_dwordx4 v42, s[28:31], s6 offen lds
v_pk_mul_f32 v[86:87], v[86:87], v[20:21] op_sel_hi:[1,0]
v_pk_mul_f32 v[48:49], v[80:81], v[80:81]
v_pk_mul_f32 v[50:51], v[82:83], v[82:83]
ds_read_b128 a[196:199], v7 offset:0
v_pk_mul_f32 v[52:53], v[84:85], v[84:85]
v_pk_mul_f32 v[54:55], v[86:87], v[86:87]
v_pk_fma_f32 v[48:49], v[48:49], s[80:81], v[10:11] op_sel_hi:[1,1,0]
buffer_load_dwordx4 v4, s[28:31], s6 offen offset:1024 lds
v_pk_fma_f32 v[50:51], v[50:51], s[80:81], v[10:11] op_sel_hi:[1,1,0]
v_pk_fma_f32 v[52:53], v[52:53], s[80:81], v[10:11] op_sel_hi:[1,1,0]
v_pk_fma_f32 v[54:55], v[54:55], s[80:81], v[10:11] op_sel_hi:[1,1,0]
ds_read_b128 a[200:203], v6 offset:2048
v_pk_mul_f32 v[48:49], v[48:49], v[80:81]
v_pk_mul_f32 v[50:51], v[50:51], v[82:83]
v_pk_mul_f32 v[52:53], v[52:53], v[84:85]
ds_read_b128 a[204:207], v7 offset:2048
v_pk_mul_f32 v[54:55], v[54:55], v[86:87]
v_exp_f32 v48, v48
v_exp_f32 v49, v49
s_waitcnt lgkmcnt(10)
s_add_u32 m0, s58, 63488
s_add_u32 s6, s6, 128
buffer_load_dwordx4 v42, s[28:31], s6 offen lds
v_exp_f32 v50, v50
v_exp_f32 v51, v51
v_exp_f32 v52, v52
ds_read_b128 a[208:211], v6 offset:4096
v_exp_f32 v53, v53
v_exp_f32 v54, v54
v_exp_f32 v55, v55
buffer_load_dwordx4 v4, s[28:31], s6 offen offset:1024 lds
v_pk_add_f32 v[48:49], v[48:49], 1.0 op_sel_hi:[1,0]
v_pk_add_f32 v[50:51], v[50:51], 1.0 op_sel_hi:[1,0]
v_pk_add_f32 v[52:53], v[52:53], 1.0 op_sel_hi:[1,0]
ds_read_b128 a[212:215], v7 offset:4096
v_pk_add_f32 v[54:55], v[54:55], 1.0 op_sel_hi:[1,0]
v_rcp_f32 v48, v48
v_rcp_f32 v49, v49
s_waitcnt lgkmcnt(10)
s_add_u32 m0, s58, 65536
s_add_u32 s6, s6, 128
buffer_load_dwordx4 v42, s[28:31], s6 offen lds
v_rcp_f32 v50, v50
v_rcp_f32 v51, v51
v_rcp_f32 v52, v52
ds_read_b128 a[216:219], v6 offset:6144
v_rcp_f32 v53, v53
v_rcp_f32 v54, v54
v_rcp_f32 v55, v55
ds_read_b128 a[220:223], v7 offset:6144
v_pk_mul_f32 v[80:81], v[80:81], v[48:49]
v_pk_mul_f32 v[82:83], v[82:83], v[50:51]
v_pk_mul_f32 v[84:85], v[84:85], v[52:53]
buffer_load_dwordx4 v4, s[28:31], s6 offen offset:1024 lds
v_pk_mul_f32 v[86:87], v[86:87], v[54:55]
v_pk_mul_f32 v[88:89], v[88:89], v[20:21] op_sel_hi:[1,0]
v_pk_mul_f32 v[90:91], v[90:91], v[20:21] op_sel_hi:[1,0]
v_pk_mul_f32 v[92:93], v[92:93], v[20:21] op_sel_hi:[1,0]
ds_read_b128 a[32:35], v6 offset:8192
v_pk_mul_f32 v[94:95], v[94:95], v[20:21] op_sel_hi:[1,0]
v_pk_mul_f32 v[48:49], v[88:89], v[88:89]
v_pk_mul_f32 v[50:51], v[90:91], v[90:91]
s_waitcnt lgkmcnt(11)
s_add_u32 m0, s58, 67584
s_add_u32 s6, s6, 128
buffer_load_dwordx4 v42, s[28:31], s6 offen lds
v_pk_mul_f32 v[52:53], v[92:93], v[92:93]
v_pk_mul_f32 v[54:55], v[94:95], v[94:95]
v_pk_fma_f32 v[48:49], v[48:49], s[80:81], v[10:11] op_sel_hi:[1,1,0]
ds_read_b128 a[36:39], v7 offset:8192
v_pk_fma_f32 v[50:51], v[50:51], s[80:81], v[10:11] op_sel_hi:[1,1,0]
v_pk_fma_f32 v[52:53], v[52:53], s[80:81], v[10:11] op_sel_hi:[1,1,0]
v_pk_fma_f32 v[54:55], v[54:55], s[80:81], v[10:11] op_sel_hi:[1,1,0]
buffer_load_dwordx4 v4, s[28:31], s6 offen offset:1024 lds
v_pk_mul_f32 v[48:49], v[48:49], v[88:89]
v_pk_mul_f32 v[50:51], v[50:51], v[90:91]
v_pk_mul_f32 v[52:53], v[52:53], v[92:93]
ds_read_b128 a[40:43], v6 offset:10240
v_pk_mul_f32 v[54:55], v[54:55], v[94:95]
v_exp_f32 v48, v48
v_exp_f32 v49, v49
ds_read_b128 a[44:47], v7 offset:10240
v_exp_f32 v50, v50
v_exp_f32 v51, v51
v_exp_f32 v52, v52
s_waitcnt lgkmcnt(12)
s_add_u32 m0, s58, 69632
s_add_u32 s6, s49, 3
s_and_b32 s6, s6, 3
s_lshl_b32 s6, s6, 16
s_add_u32 s6, s6, s87
s_xor_b32 s7, s88, 512
s_add_u32 s6, s6, s7
buffer_load_dwordx4 v42, s[28:31], s6 offen lds
v_exp_f32 v53, v53
v_exp_f32 v54, v54
v_exp_f32 v55, v55
s_waitcnt vmcnt(22)
ds_read_b128 a[48:51], v6 offset:12288
v_pk_add_f32 v[48:49], v[48:49], 1.0 op_sel_hi:[1,0]
v_pk_add_f32 v[50:51], v[50:51], 1.0 op_sel_hi:[1,0]
v_pk_add_f32 v[52:53], v[52:53], 1.0 op_sel_hi:[1,0]
buffer_load_dwordx4 v4, s[28:31], s6 offen offset:1024 lds
v_pk_add_f32 v[54:55], v[54:55], 1.0 op_sel_hi:[1,0]
v_rcp_f32 v48, v48
v_rcp_f32 v49, v49
ds_read_b128 a[52:55], v7 offset:12288
v_rcp_f32 v50, v50
v_rcp_f32 v51, v51
v_rcp_f32 v52, v52
v_rcp_f32 v53, v53
v_rcp_f32 v54, v54
v_rcp_f32 v55, v55
v_pk_mul_f32 v[88:89], v[88:89], v[48:49]
v_pk_mul_f32 v[90:91], v[90:91], v[50:51]
v_pk_mul_f32 v[92:93], v[92:93], v[52:53]
v_pk_mul_f32 v[94:95], v[94:95], v[54:55]
v_cvt_pk_bf16_f32 v152, v80, v81
v_cvt_pk_bf16_f32 v153, v82, v83
v_cvt_pk_bf16_f32 v156, v84, v85
v_cvt_pk_bf16_f32 v157, v86, v87
v_cvt_pk_bf16_f32 v154, v88, v89
v_cvt_pk_bf16_f32 v155, v90, v91
v_cvt_pk_bf16_f32 v158, v92, v93
v_cvt_pk_bf16_f32 v159, v94, v95
buffer_store_dwordx4 v[152:155], v18, s[16:19], s59 offen offset:1024 sc1
buffer_store_dwordx4 v[156:159], v18, s[16:19], s59 offen offset:1536 sc1
s_waitcnt lgkmcnt(12)
s_add_u32 m0, s58, 0
s_add_u32 s6, s49, 3
s_and_b32 s6, s6, 3
s_lshl_b32 s6, s6, 16
s_add_u32 s6, s6, s87
s_xor_b32 s7, s88, 640
s_add_u32 s6, s6, s7
buffer_load_dwordx4 v42, s[28:31], s6 offen lds
buffer_load_dwordx4 v4, s[28:31], s6 offen offset:1024 lds
s_waitcnt lgkmcnt(10)
s_add_u32 m0, s58, 2048
s_add_u32 s6, s6, 128
buffer_load_dwordx4 v42, s[28:31], s6 offen lds
buffer_load_dwordx4 v4, s[28:31], s6 offen offset:1024 lds
s_waitcnt lgkmcnt(8)
s_add_u32 m0, s58, 4096
s_add_u32 s6, s6, 128
buffer_load_dwordx4 v42, s[28:31], s6 offen lds
buffer_load_dwordx4 v4, s[28:31], s6 offen offset:1024 lds
s_branch .Lk_p4
.Lk_idle:
s_waitcnt vmcnt(0)
s_barrier
v_and_b32 v48, 1, v22
v_lshl_add_u32 v48, v48, 7, v23
s_and_b32 exec_lo, 0xffff0000, s3
s_mov_b32 exec_hi, 0
buffer_atomic_add v3, v48, s[16:19], 0 offen offset:384
s_mov_b64 exec, -1
v_xor_b32 v48, v21, v22
v_and_b32 v48, 1, v48
v_cmp_ne_u32_e64 s[76:77], 0, v48
s_nop 0
s_and_b32 s76, s76, 0xffff
s_and_b32 s76, s76, s3
s_cbranch_scc0 .Lk_fresh_14
v_and_b32 v48, 1, v21
v_lshl_add_u32 v48, v48, 7, v23
s_mov_b32 exec_lo, s76
s_mov_b32 exec_hi, 0
buffer_atomic_add v3, v48, s[16:19], 0 offen offset:128
s_waitcnt vmcnt(0)
s_mov_b64 exec, -1
.Lk_fresh_14:
s_cmp_lt_u32 s50, 14
s_cbranch_scc0 .Lk_nocs_15
s_and_b32 s69, s64, 1
s_mul_i32 s69, s69, 7
s_add_u32 s69, s69, s50
s_cmp_ge_u32 s50, 7
s_cselect_b32 s70, 7, 0
s_add_u32 s69, s69, s70
s_mul_i32 s67, s49, 401408
s_lshl_b32 s68, s69, 7
s_add_u32 s67, s67, s68
v_lshrrev_b32 v48, 3, v0
v_mul_u32_u24 v48, 3584, v48
v_and_b32 v49, 7, v0
v_lshl_add_u32 v48, v49, 4, v48
buffer_load_dwordx4 v[64:67], v48, s[24:27], s67 offen
s_add_u32 s67, s67, 28672
buffer_load_dwordx4 v[68:71], v48, s[24:27], s67 offen
s_add_u32 s67, s67, 28672
buffer_load_dwordx4 v[72:75], v48, s[24:27], s67 offen
s_add_u32 s67, s67, 28672
buffer_load_dwordx4 v[76:79], v48, s[24:27], s67 offen
s_add_u32 s67, s67, 28672
buffer_load_dwordx4 v[80:83], v48, s[24:27], s67 offen
s_add_u32 s67, s67, 28672
buffer_load_dwordx4 v[84:87], v48, s[24:27], s67 offen
s_add_u32 s67, s67, 28672
buffer_load_dwordx4 v[88:91], v48, s[24:27], s67 offen
s_add_u32 s67, s67, 28672
buffer_load_dwordx4 v[92:95], v48, s[24:27], s67 offen
s_add_u32 s67, s67, 28672
buffer_load_dwordx4 v[96:99], v48, s[24:27], s67 offen
s_add_u32 s67, s67, 28672
buffer_load_dwordx4 v[100:103], v48, s[24:27], s67 offen
s_add_u32 s67, s67, 28672
buffer_load_dwordx4 v[104:107], v48, s[24:27], s67 offen
s_add_u32 s67, s67, 28672
buffer_load_dwordx4 v[108:111], v48, s[24:27], s67 offen
s_add_u32 s67, s67, 28672
buffer_load_dwordx4 v[112:115], v48, s[24:27], s67 offen
s_add_u32 s67, s67, 28672
buffer_load_dwordx4 v[116:119], v48, s[24:27], s67 offen
s_add_u32 s67, s67, 28672
v_mov_b32 v120, 0
v_mov_b32 v121, 0
v_mov_b32 v122, 0
v_mov_b32 v123, 0
v_mov_b32 v124, 0
v_mov_b32 v125, 0
v_mov_b32 v126, 0
v_mov_b32 v127, 0
s_mov_b32 s67, 0x3f80
s_mov_b32 s68, 0x3f800000
s_waitcnt vmcnt(13)
v_dot2c_f32_bf16 v120, s67, v64
v_dot2c_f32_bf16 v121, s68, v64
v_dot2c_f32_bf16 v122, s67, v65
v_dot2c_f32_bf16 v123, s68, v65
v_dot2c_f32_bf16 v124, s67, v66
v_dot2c_f32_bf16 v125, s68, v66
v_dot2c_f32_bf16 v126, s67, v67
v_dot2c_f32_bf16 v127, s68, v67
s_waitcnt vmcnt(12)
v_dot2c_f32_bf16 v120, s67, v68
v_dot2c_f32_bf16 v121, s68, v68
v_dot2c_f32_bf16 v122, s67, v69
v_dot2c_f32_bf16 v123, s68, v69
v_dot2c_f32_bf16 v124, s67, v70
v_dot2c_f32_bf16 v125, s68, v70
v_dot2c_f32_bf16 v126, s67, v71
v_dot2c_f32_bf16 v127, s68, v71
s_waitcnt vmcnt(11)
v_dot2c_f32_bf16 v120, s67, v72
v_dot2c_f32_bf16 v121, s68, v72
v_dot2c_f32_bf16 v122, s67, v73
v_dot2c_f32_bf16 v123, s68, v73
v_dot2c_f32_bf16 v124, s67, v74
v_dot2c_f32_bf16 v125, s68, v74
v_dot2c_f32_bf16 v126, s67, v75
v_dot2c_f32_bf16 v127, s68, v75
s_waitcnt vmcnt(10)
v_dot2c_f32_bf16 v120, s67, v76
v_dot2c_f32_bf16 v121, s68, v76
v_dot2c_f32_bf16 v122, s67, v77
v_dot2c_f32_bf16 v123, s68, v77
v_dot2c_f32_bf16 v124, s67, v78
v_dot2c_f32_bf16 v125, s68, v78
v_dot2c_f32_bf16 v126, s67, v79
v_dot2c_f32_bf16 v127, s68, v79
s_waitcnt vmcnt(9)
v_dot2c_f32_bf16 v120, s67, v80
v_dot2c_f32_bf16 v121, s68, v80
v_dot2c_f32_bf16 v122, s67, v81
v_dot2c_f32_bf16 v123, s68, v81
v_dot2c_f32_bf16 v124, s67, v82
v_dot2c_f32_bf16 v125, s68, v82
v_dot2c_f32_bf16 v126, s67, v83
v_dot2c_f32_bf16 v127, s68, v83
s_waitcnt vmcnt(8)
v_dot2c_f32_bf16 v120, s67, v84
v_dot2c_f32_bf16 v121, s68, v84
v_dot2c_f32_bf16 v122, s67, v85
v_dot2c_f32_bf16 v123, s68, v85
v_dot2c_f32_bf16 v124, s67, v86
v_dot2c_f32_bf16 v125, s68, v86
v_dot2c_f32_bf16 v126, s67, v87
v_dot2c_f32_bf16 v127, s68, v87
s_waitcnt vmcnt(7)
v_dot2c_f32_bf16 v120, s67, v88
v_dot2c_f32_bf16 v121, s68, v88
v_dot2c_f32_bf16 v122, s67, v89
v_dot2c_f32_bf16 v123, s68, v89
v_dot2c_f32_bf16 v124, s67, v90
v_dot2c_f32_bf16 v125, s68, v90
v_dot2c_f32_bf16 v126, s67, v91
v_dot2c_f32_bf16 v127, s68, v91
s_waitcnt vmcnt(6)
v_dot2c_f32_bf16 v120, s67, v92
v_dot2c_f32_bf16 v121, s68, v92
v_dot2c_f32_bf16 v122, s67, v93
v_dot2c_f32_bf16 v123, s68, v93
v_dot2c_f32_bf16 v124, s67, v94
v_dot2c_f32_bf16 v125, s68, v94
v_dot2c_f32_bf16 v126, s67, v95
v_dot2c_f32_bf16 v127, s68, v95
s_waitcnt vmcnt(5)
v_dot2c_f32_bf16 v120, s67, v96
v_dot2c_f32_bf16 v121, s68, v96
v_dot2c_f32_bf16 v122, s67, v97
v_dot2c_f32_bf16 v123, s68, v97
v_dot2c_f32_bf16 v124, s67, v98
v_dot2c_f32_bf16 v125, s68, v98
v_dot2c_f32_bf16 v126, s67, v99
v_dot2c_f32_bf16 v127, s68, v99
s_waitcnt vmcnt(4)
v_dot2c_f32_bf16 v120, s67, v100
v_dot2c_f32_bf16 v121, s68, v100
v_dot2c_f32_bf16 v122, s67, v101
v_dot2c_f32_bf16 v123, s68, v101
v_dot2c_f32_bf16 v124, s67, v102
v_dot2c_f32_bf16 v125, s68, v102
v_dot2c_f32_bf16 v126, s67, v103
v_dot2c_f32_bf16 v127, s68, v103
s_waitcnt vmcnt(3)
v_dot2c_f32_bf16 v120, s67, v104
v_dot2c_f32_bf16 v121, s68, v104
v_dot2c_f32_bf16 v122, s67, v105
v_dot2c_f32_bf16 v123, s68, v105
v_dot2c_f32_bf16 v124, s67, v106
v_dot2c_f32_bf16 v125, s68, v106
v_dot2c_f32_bf16 v126, s67, v107
v_dot2c_f32_bf16 v127, s68, v107
s_waitcnt vmcnt(2)
v_dot2c_f32_bf16 v120, s67, v108
v_dot2c_f32_bf16 v121, s68, v108
v_dot2c_f32_bf16 v122, s67, v109
v_dot2c_f32_bf16 v123, s68, v109
v_dot2c_f32_bf16 v124, s67, v110
v_dot2c_f32_bf16 v125, s68, v110
v_dot2c_f32_bf16 v126, s67, v111
v_dot2c_f32_bf16 v127, s68, v111
s_waitcnt vmcnt(1)
v_dot2c_f32_bf16 v120, s67, v112
v_dot2c_f32_bf16 v121, s68, v112
v_dot2c_f32_bf16 v122, s67, v113
v_dot2c_f32_bf16 v123, s68, v113
v_dot2c_f32_bf16 v124, s67, v114
v_dot2c_f32_bf16 v125, s68, v114
v_dot2c_f32_bf16 v126, s67, v115
v_dot2c_f32_bf16 v127, s68, v115
s_waitcnt vmcnt(0)
v_dot2c_f32_bf16 v120, s67, v116
v_dot2c_f32_bf16 v121, s68, v116
v_dot2c_f32_bf16 v122, s67, v117
v_dot2c_f32_bf16 v123, s68, v117
v_dot2c_f32_bf16 v124, s67, v118
v_dot2c_f32_bf16 v125, s68, v118
v_dot2c_f32_bf16 v126, s67, v119
v_dot2c_f32_bf16 v127, s68, v119
v_permlane32_swap_b32 v120, v124
v_permlane32_swap_b32 v121, v125
v_permlane32_swap_b32 v122, v126
v_permlane32_swap_b32 v123, v127
v_add_f32 v120, v120, v124
v_add_f32 v121, v121, v125
v_add_f32 v122, v122, v126
v_add_f32 v123, v123, v127
s_nop 0
v_permlane16_swap_b32 v120, v122
v_permlane16_swap_b32 v121, v123
v_add_f32 v120, v120, v122
v_add_f32 v121, v121, v123
s_nop 0
v_add_f32_dpp v120, v120, v120 row_ror:8 row_mask:0xf bank_mask:0xf
v_add_f32_dpp v121, v121, v121 row_ror:8 row_mask:0xf bank_mask:0xf
v_bfe_u32 v50, v0, 3, 1
v_cmp_eq_u32_e64 s[76:77], 1, v50
v_bfe_u32 v51, v0, 4, 1
v_lshl_add_u32 v50, v51, 1, v50
v_lshrrev_b32 v51, 5, v0
v_lshl_add_u32 v50, v51, 2, v50
v_lshl_add_u32 v50, v49, 3, v50
v_cndmask_b32_e64 v120, v120, v121, s[76:77]
s_lshl2_add_u32 s67, s49, 162304
v_lshl_add_u32 v50, v50, 4, s67
ds_write_b32 v50, v120 offset:0
v_add_u32 v50, 162304, v1
s_waitcnt lgkmcnt(0)
s_barrier
ds_read_b128 v[60:63], v50 offset:0
s_lshl_b32 s69, s69, 8
s_add_u32 s69, s69, 1703936
v_lshlrev_b32 v48, 2, v0
s_waitcnt lgkmcnt(0)
v_add_f32 v60, v60, v61
v_add_f32 v62, v62, v63
v_add_f32 v60, v60, v62
v_cvt_pk_bf16_f32 v50, v60, v60
v_lshlrev_b32 v50, 16, v50
v_sub_f32 v51, v60, v50
v_cvt_pk_bf16_f32 v50, v50, v51
v_and_b32 v50, 0xfffeffff, v50
buffer_store_dword v50, v48, s[16:19], s69 offen sc1
.Lk_nocs_15:
s_and_b32 s71, s52, 6
s_lshl2_add_u32 s71, s71, s49
s_add_u32 m0, s57, 0
s_add_u32 s6, s49, 0
s_and_b32 s6, s6, 3
s_lshl_b32 s6, s6, 16
s_add_u32 s6, s6, s87
s_xor_b32 s7, s88, 0
s_add_u32 s6, s6, s7
buffer_load_dwordx4 v42, s[28:31], s6 offen lds
buffer_load_dwordx4 v4, s[28:31], s6 offen offset:1024 lds
s_add_u32 m0, s57, 2048
s_add_u32 s6, s6, 128
buffer_load_dwordx4 v42, s[28:31], s6 offen lds
buffer_load_dwordx4 v4, s[28:31], s6 offen offset:1024 lds
s_add_u32 m0, s57, 4096
s_add_u32 s6, s6, 128
buffer_load_dwordx4 v42, s[28:31], s6 offen lds
buffer_load_dwordx4 v4, s[28:31], s6 offen offset:1024 lds
s_add_u32 m0, s57, 6144
s_add_u32 s6, s6, 128
buffer_load_dwordx4 v42, s[28:31], s6 offen lds
buffer_load_dwordx4 v4, s[28:31], s6 offen offset:1024 lds
s_add_u32 m0, s57, 8192
s_add_u32 s6, s49, 1
s_and_b32 s6, s6, 3
s_lshl_b32 s6, s6, 16
s_add_u32 s6, s6, s87
s_xor_b32 s7, s88, 0
s_add_u32 s6, s6, s7
buffer_load_dwordx4 v42, s[28:31], s6 offen lds
buffer_load_dwordx4 v4, s[28:31], s6 offen offset:1024 lds
s_add_u32 m0, s58, 57344
s_add_u32 s6, s6, 128
buffer_load_dwordx4 v42, s[28:31], s6 offen lds
buffer_load_dwordx4 v4, s[28:31], s6 offen offset:1024 lds
s_add_u32 m0, s58, 59392
s_add_u32 s6, s6, 128
buffer_load_dwordx4 v42, s[28:31], s6 offen lds
buffer_load_dwordx4 v4, s[28:31], s6 offen offset:1024 lds
s_add_u32 m0, s58, 61440
s_add_u32 s6, s6, 128
buffer_load_dwordx4 v42, s[28:31], s6 offen lds
buffer_load_dwordx4 v4, s[28:31], s6 offen offset:1024 lds
s_add_u32 m0, s58, 63488
s_add_u32 s6, s49, 2
s_and_b32 s6, s6, 3
s_lshl_b32 s6, s6, 16
s_add_u32 s6, s6, s87
s_xor_b32 s7, s88, 0
s_add_u32 s6, s6, s7
buffer_load_dwordx4 v42, s[28:31], s6 offen lds
buffer_load_dwordx4 v4, s[28:31], s6 offen offset:1024 lds
s_add_u32 m0, s58, 65536
s_add_u32 s6, s6, 128
buffer_load_dwordx4 v42, s[28:31], s6 offen lds
buffer_load_dwordx4 v4, s[28:31], s6 offen offset:1024 lds
s_add_u32 m0, s58, 67584
s_add_u32 s6, s6, 128
buffer_load_dwordx4 v42, s[28:31], s6 offen lds
buffer_load_dwordx4 v4, s[28:31], s6 offen offset:1024 lds
s_add_u32 m0, s58, 69632
s_add_u32 s6, s6, 128
buffer_load_dwordx4 v42, s[28:31], s6 offen lds
buffer_load_dwordx4 v4, s[28:31], s6 offen offset:1024 lds
s_add_u32 m0, s58, 0
s_add_u32 s6, s49, 3
s_and_b32 s6, s6, 3
s_lshl_b32 s6, s6, 16
s_add_u32 s6, s6, s87
s_xor_b32 s7, s88, 0
s_add_u32 s6, s6, s7
buffer_load_dwordx4 v42, s[28:31], s6 offen lds
buffer_load_dwordx4 v4, s[28:31], s6 offen offset:1024 lds
s_add_u32 m0, s58, 2048
s_add_u32 s6, s6, 128
buffer_load_dwordx4 v42, s[28:31], s6 offen lds
buffer_load_dwordx4 v4, s[28:31], s6 offen offset:1024 lds
s_add_u32 m0, s58, 4096
s_add_u32 s6, s6, 128
buffer_load_dwordx4 v42, s[28:31], s6 offen lds
buffer_load_dwordx4 v4, s[28:31], s6 offen offset:1024 lds
s_add_u32 m0, s58, 6144
s_add_u32 s6, s6, 128
buffer_load_dwordx4 v42, s[28:31], s6 offen lds
buffer_load_dwordx4 v4, s[28:31], s6 offen offset:1024 lds
s_add_u32 m0, s58, 8192
s_add_u32 s6, s49, 0
s_and_b32 s6, s6, 3
s_lshl_b32 s6, s6, 16
s_add_u32 s6, s6, s87
s_xor_b32 s7, s88, 512
s_add_u32 s6, s6, s7
buffer_load_dwordx4 v42, s[28:31], s6 offen lds
buffer_load_dwordx4 v4, s[28:31], s6 offen offset:1024 lds
s_add_u32 m0, s58, 10240
s_add_u32 s6, s6, 128
buffer_load_dwordx4 v42, s[28:31], s6 offen lds
buffer_load_dwordx4 v4, s[28:31], s6 offen offset:1024 lds
s_add_u32 m0, s58, 12288
s_add_u32 s6, s6, 128
buffer_load_dwordx4 v42, s[28:31], s6 offen lds
buffer_load_dwordx4 v4, s[28:31], s6 offen offset:1024 lds
s_and_b32 exec_lo, 1, s3
s_mov_b32 exec_hi, 0
s_mov_b32 s66, 32768
.Lk_poll_16:
buffer_load_dword v2, off, s[16:19], s63 sc1
s_waitcnt vmcnt(0)
v_cmp_ne_u32_e32 vcc, s62, v2
s_nop 4
s_cbranch_vccz .Lk_done_17
s_sub_u32 s66, s66, 1
s_cbranch_scc0 .Lk_poll_16
.Lk_done_17:
s_mov_b64 exec, -1
s_barrier
s_and_b32 exec_lo, 0xffff0000, s3
s_mov_b32 exec_hi, 0
buffer_load_dword v21, v23, s[16:19], 0 offen sc1
s_mov_b64 exec, -1
s_add_u32 s7, s57, 0
v_add_u32 v6, s7, v5
v_xor_b32 v7, 64, v6
ds_read_b128 a[0:3], v6 offset:0
ds_read_b128 a[4:7], v7 offset:0
ds_read_b128 a[8:11], v6 offset:2048
ds_read_b128 a[12:15], v7 offset:2048
ds_read_b128 a[16:19], v6 offset:4096
ds_read_b128 a[20:23], v7 offset:4096
ds_read_b128 a[24:27], v6 offset:6144
ds_read_b128 a[28:31], v7 offset:6144
ds_read_b128 a[64:67], v6 offset:8192
ds_read_b128 a[68:71], v7 offset:8192
s_add_u32 s7, s58, 57344
v_add_u32 v6, s7, v5
v_xor_b32 v7, 64, v6
ds_read_b128 a[72:75], v6 offset:0
ds_read_b128 a[76:79], v7 offset:0
ds_read_b128 a[80:83], v6 offset:2048
ds_read_b128 a[84:87], v7 offset:2048
ds_read_b128 a[88:91], v6 offset:4096
ds_read_b128 a[92:95], v7 offset:4096
ds_read_b128 a[128:131], v6 offset:6144
ds_read_b128 a[132:135], v7 offset:6144
ds_read_b128 a[136:139], v6 offset:8192
ds_read_b128 a[140:143], v7 offset:8192
ds_read_b128 a[144:147], v6 offset:10240
ds_read_b128 a[148:151], v7 offset:10240
ds_read_b128 a[152:155], v6 offset:12288
ds_read_b128 a[156:159], v7 offset:12288
s_add_u32 s7, s58, 0
v_add_u32 v6, s7, v5
v_xor_b32 v7, 64, v6
ds_read_b128 a[192:195], v6 offset:0
ds_read_b128 a[196:199], v7 offset:0
ds_read_b128 a[200:203], v6 offset:2048
ds_read_b128 a[204:207], v7 offset:2048
ds_read_b128 a[208:211], v6 offset:4096
ds_read_b128 a[212:215], v7 offset:4096
ds_read_b128 a[216:219], v6 offset:6144
ds_read_b128 a[220:223], v7 offset:6144
ds_read_b128 a[32:35], v6 offset:8192
ds_read_b128 a[36:39], v7 offset:8192
ds_read_b128 a[40:43], v6 offset:10240
ds_read_b128 a[44:47], v7 offset:10240
ds_read_b128 a[48:51], v6 offset:12288
ds_read_b128 a[52:55], v7 offset:12288
s_waitcnt vmcnt(0)
v_xor_b32 v48, v21, v22
v_and_b32 v48, 1, v48
v_cmp_ne_u32_e64 s[76:77], 0, v48
s_nop 0
s_and_b32 s76, s76, 0xffff0000
s_and_b32 s76, s76, s3
s_cbranch_scc0 .Lk_fresh_18
v_and_b32 v48, 1, v21
v_lshl_add_u32 v48, v48, 7, v23
s_mov_b32 exec_lo, s76
s_mov_b32 exec_hi, 0
buffer_atomic_add v3, v48, s[16:19], 0 offen offset:384
s_waitcnt vmcnt(0)
s_mov_b64 exec, -1
.Lk_fresh_18:
s_waitcnt lgkmcnt(14)
s_add_u32 m0, s57, 4096
s_add_u32 s6, s49, 0
s_and_b32 s6, s6, 3
s_lshl_b32 s6, s6, 16
s_add_u32 s6, s6, s87
s_xor_b32 s7, s88, 896
s_add_u32 s6, s6, s7
buffer_load_dwordx4 v42, s[28:31], s6 offen lds
buffer_load_dwordx4 v4, s[28:31], s6 offen offset:1024 lds
s_add_u32 m0, s57, 6144
s_add_u32 s6, s49, 1
s_and_b32 s6, s6, 3
s_lshl_b32 s6, s6, 16
s_add_u32 s6, s6, s87
s_xor_b32 s7, s88, 512
s_add_u32 s6, s6, s7
buffer_load_dwordx4 v42, s[28:31], s6 offen lds
buffer_load_dwordx4 v4, s[28:31], s6 offen offset:1024 lds
s_add_u32 m0, s57, 8192
s_add_u32 s6, s6, 128
buffer_load_dwordx4 v42, s[28:31], s6 offen lds
buffer_load_dwordx4 v4, s[28:31], s6 offen offset:1024 lds
s_add_u32 m0, s58, 57344
s_add_u32 s6, s6, 128
buffer_load_dwordx4 v42, s[28:31], s6 offen lds
buffer_load_dwordx4 v4, s[28:31], s6 offen offset:1024 lds
s_add_u32 m0, s58, 59392
s_add_u32 s6, s6, 128
buffer_load_dwordx4 v42, s[28:31], s6 offen lds
buffer_load_dwordx4 v4, s[28:31], s6 offen offset:1024 lds
s_add_u32 m0, s58, 61440
s_add_u32 s6, s49, 2
s_and_b32 s6, s6, 3
s_lshl_b32 s6, s6, 16
s_add_u32 s6, s6, s87
s_xor_b32 s7, s88, 512
s_add_u32 s6, s6, s7
buffer_load_dwordx4 v42, s[28:31], s6 offen lds
buffer_load_dwordx4 v4, s[28:31], s6 offen offset:1024 lds
s_add_u32 m0, s58, 63488
s_add_u32 s6, s6, 128
buffer_load_dwordx4 v42, s[28:31], s6 offen lds
buffer_load_dwordx4 v4, s[28:31], s6 offen offset:1024 lds
s_add_u32 m0, s58, 65536
s_add_u32 s6, s6, 128
buffer_load_dwordx4 v42, s[28:31], s6 offen lds
buffer_load_dwordx4 v4, s[28:31], s6 offen offset:1024 lds
s_add_u32 m0, s58, 67584
s_add_u32 s6, s6, 128
buffer_load_dwordx4 v42, s[28:31], s6 offen lds
buffer_load_dwordx4 v4, s[28:31], s6 offen offset:1024 lds
s_add_u32 m0, s58, 69632
s_add_u32 s6, s49, 3
s_and_b32 s6, s6, 3
s_lshl_b32 s6, s6, 16
s_add_u32 s6, s6, s87
s_xor_b32 s7, s88, 512
s_add_u32 s6, s6, s7
buffer_load_dwordx4 v42, s[28:31], s6 offen lds
buffer_load_dwordx4 v4, s[28:31], s6 offen offset:1024 lds
s_waitcnt lgkmcnt(12)
s_add_u32 m0, s58, 0
s_add_u32 s6, s6, 128
buffer_load_dwordx4 v42, s[28:31], s6 offen lds
buffer_load_dwordx4 v4, s[28:31], s6 offen offset:1024 lds
s_waitcnt lgkmcnt(10)
s_add_u32 m0, s58, 2048
s_add_u32 s6, s6, 128
buffer_load_dwordx4 v42, s[28:31], s6 offen lds
buffer_load_dwordx4 v4, s[28:31], s6 offen offset:1024 lds
s_waitcnt lgkmcnt(8)
s_add_u32 m0, s58, 4096
s_add_u32 s6, s6, 128
buffer_load_dwordx4 v42, s[28:31], s6 offen lds
buffer_load_dwordx4 v4, s[28:31], s6 offen offset:1024 lds
s_mov_b32 s78, 0
v_lshlrev_b32 v48, 7, v0
s_and_b32 exec_lo, -1, s3
s_mov_b32 exec_hi, 0
buffer_load_dword v48, v48, s[44:47], 0 offen
s_mov_b64 exec, -1
s_and_b32 s68, s48, 7
s_lshr_b32 s69, s48, 6
s_lshl2_add_u32 s68, s68, s69
s_lshl_b32 s68, s68, 7
s_add_u32 s68, s68, 1050624
s_and_b32 exec_lo, 1, s3
s_mov_b32 exec_hi, 0
s_mov_b32 s66, 32768
.Lk_poll_19:
buffer_load_dword v2, off, s[16:19], s68 sc1
s_waitcnt vmcnt(0)
v_cmp_ne_u32_e32 vcc, 1, v2
s_nop 4
s_cbranch_vccz .Lk_done_20
s_sub_u32 s66, s66, 1
s_cbranch_scc0 .Lk_poll_19
.Lk_done_20:
s_mov_b64 exec, -1
s_barrier
s_sleep 6
.Lk_p4:
s_mul_i32 s69, s53, 7
s_and_b32 s70, s53, 1
s_add_u32 s71, s69, 6
s_cmp_eq_u32 s70, 1
s_cselect_b32 s90, s69, s71
s_add_u32 s89, s69, s70
s_lshl2_add_u32 s71, s52, s49
s_lshl_b32 s60, s52, 7
s_lshl_b32 s61, s71, 5
s_mul_i32 s71, s71, 28
s_lshr_b32 s72, s89, 1
s_add_u32 s72, s72, s71
s_lshl_b32 s96, s72, 10
s_lshr_b32 s72, s90, 1
s_add_u32 s72, s72, s71
s_lshl_b32 s72, s72, 10
s_lshl3_add_u32 s97, s70, s72
s_add_u32 s67, s49, s53
s_and_b32 s67, s67, 3
s_lshl_b32 s68, s67, 14
s_lshl_b32 s69, s88, 4
s_add_u32 s68, s68, s69
s_add_u32 s68, s68, 8388608
s_lshl_b32 s69, s69, 1
s_sub_u32 s56, 8192, s69
s_lshl_b32 s89, s89, 16
s_add_u32 s89, s89, s68
s_lshl_b32 s90, s90, 16
s_add_u32 s90, s90, s68
s_add_u32 s68, s53, 1
s_bitcmp1_b32 s68, 1
s_cselect_b32 s63, 3, 2
s_lshr_b32 s68, s52, 3
s_lshl3_add_u32 s68, s68, s53
s_lshl_b32 s68, s68, 11
s_add_u32 s68, s68, 1114112
s_add_u32 s67, s49, 1
s_and_b32 s67, s67, 3
s_mul_i32 s67, s67, 3072
s_add_u32 s67, s67, 131072
v_add_u32 v8, s67, v1
s_add_u32 s67, s49, 2
s_and_b32 s67, s67, 3
s_mul_i32 s67, s67, 3072
s_add_u32 s67, s67, 132096
v_add_u32 v9, s67, v1
s_add_u32 s67, s49, 3
s_and_b32 s67, s67, 3
s_mul_i32 s67, s67, 3072
s_add_u32 s67, s67, 133120
v_add_u32 v10, s67, v1
s_mul_i32 s67, s49, 3072
s_add_u32 s67, s67, 131072
v_add_u32 v11, s67, v1
v_and_b32 v13, 15, v0
v_lshlrev_b32 v13, 1, v13
buffer_load_ushort v12, v13, s[44:47], s61 offen
v_lshlrev_b32 v13, 7, v0
s_waitcnt lgkmcnt(0)
s_and_b32 exec_lo, 65535, s3
s_mov_b32 exec_hi, 0
s_mov_b32 s66, 32768
.Lk_poll_21:
buffer_load_dword v2, v13, s[16:19], s68 offen sc1
s_waitcnt vmcnt(0)
v_cmp_ne_u32_e32 vcc, s63, v2
s_nop 4
s_cbranch_vccz .Lk_done_22
s_sub_u32 s66, s66, 1
s_cbranch_scc0 .Lk_poll_21
.Lk_done_22:
s_mov_b64 exec, -1
s_barrier
s_mov_b32 s72, s89
buffer_load_dwordx4 v[64:67], v1, s[16:19], s72 offen sc1
buffer_load_dwordx4 v[68:71], v1, s[16:19], s72 offen offset:1024 sc1
buffer_load_dwordx4 v[72:75], v1, s[16:19], s72 offen offset:2048 sc1
buffer_load_dwordx4 v[76:79], v1, s[16:19], s72 offen offset:3072 sc1
s_and_b32 exec_lo, s78, s3
s_mov_b32 exec_hi, 0
buffer_atomic_add v3, v16, s[16:19], 0 offen
s_mov_b64 exec, -1
s_lshr_b32 s67, s52, 1
s_lshl3_add_u32 s67, s67, s53
s_lshl_b32 s67, s67, 7
s_add_u32 s62, s67, 1064960
v_lshlrev_b32 v12, 16, v12
s_add_u32 s72, s72, 4096
buffer_load_dwordx4 v[80:83], v1, s[16:19], s72 offen sc1
s_add_u32 s7, s57, 0
v_add_u32 v6, s7, v5
v_xor_b32 v7, 64, v6
ds_read_b128 a[56:59], v6 offset:4096
ds_read_b128 a[60:63], v7 offset:4096
ds_read_b128 a[96:99], v6 offset:6144
buffer_load_dwordx4 v[84:87], v1, s[16:19], s72 offen offset:1024 sc1
ds_read_b128 a[100:103], v7 offset:6144
ds_read_b128 a[104:107], v6 offset:8192
buffer_load_dwordx4 v[88:91], v1, s[16:19], s72 offen offset:2048 sc1
ds_read_b128 a[108:111], v7 offset:8192
s_add_u32 s7, s58, 57344
v_add_u32 v6, s7, v5
v_xor_b32 v7, 64, v6
ds_read_b128 a[112:115], v6 offset:0
ds_read_b128 a[116:119], v7 offset:0
buffer_load_dwordx4 v[92:95], v1, s[16:19], s72 offen offset:3072 sc1
ds_read_b128 a[120:123], v6 offset:2048
ds_read_b128 a[124:127], v7 offset:2048
s_add_u32 s72, s89, s56
buffer_load_dwordx4 v[96:99], v1, s[16:19], s72 offen sc1
ds_read_b128 a[160:163], v6 offset:4096
ds_read_b128 a[164:167], v7 offset:4096
ds_read_b128 a[168:171], v6 offset:6144
buffer_load_dwordx4 v[100:103], v1, s[16:19], s72 offen offset:1024 sc1
ds_read_b128 a[172:175], v7 offset:6144
ds_read_b128 a[176:179], v6 offset:8192
buffer_load_dwordx4 v[104:107], v1, s[16:19], s72 offen offset:2048 sc1
ds_read_b128 a[180:183], v7 offset:8192
ds_read_b128 a[184:187], v6 offset:10240
ds_read_b128 a[188:191], v7 offset:10240
buffer_load_dwordx4 v[108:111], v1, s[16:19], s72 offen offset:3072 sc1
ds_read_b128 a[224:227], v6 offset:12288
ds_read_b128 a[228:231], v7 offset:12288
s_add_u32 s7, s58, 0
v_add_u32 v6, s7, v5
v_xor_b32 v7, 64, v6
ds_read_b128 a[232:235], v6 offset:0
ds_read_b128 a[236:239], v7 offset:0
ds_read_b128 a[240:243], v6 offset:2048
ds_read_b128 a[244:247], v7 offset:2048
ds_read_b128 a[248:251], v6 offset:4096
ds_read_b128 a[252:255], v7 offset:4096
s_waitcnt vmcnt(12)
v_mfma_f32_16x16x32_bf16 v[192:195], v[64:67], a[0:3], 0
v_mfma_f32_16x16x32_bf16 v[196:199], v[64:67], a[64:67], 0
s_add_u32 s72, s72, 4096
buffer_load_dwordx4 v[112:115], v1, s[16:19], s72 offen sc1
v_mfma_f32_16x16x32_bf16 v[200:203], v[64:67], a[128:131], 0
v_mfma_f32_16x16x32_bf16 v[204:207], v[64:67], a[192:195], 0
buffer_load_dwordx4 v[116:119], v1, s[16:19], s72 offen offset:1024 sc1
s_waitcnt vmcnt(13)
v_mfma_f32_16x16x32_bf16 v[192:195], v[68:71], a[4:7], v[192:195]
v_mfma_f32_16x16x32_bf16 v[196:199], v[68:71], a[68:71], v[196:199]
buffer_load_dwordx4 v[120:123], v1, s[16:19], s72 offen offset:2048 sc1
v_mfma_f32_16x16x32_bf16 v[200:203], v[68:71], a[132:135], v[200:203]
v_mfma_f32_16x16x32_bf16 v[204:207], v[68:71], a[196:199], v[204:207]
buffer_load_dwordx4 v[124:127], v1, s[16:19], s72 offen offset:3072 sc1
s_waitcnt vmcnt(14)
v_mfma_f32_16x16x32_bf16 v[192:195], v[72:75], a[8:11], v[192:195]
v_mfma_f32_16x16x32_bf16 v[196:199], v[72:75], a[72:75], v[196:199]
s_add_u32 s89, s89, 65536
s_mov_b32 s72, s89
buffer_load_dwordx4 v[64:67], v1, s[16:19], s72 offen sc1
v_mfma_f32_16x16x32_bf16 v[200:203], v[72:75], a[136:139], v[200:203]
v_mfma_f32_16x16x32_bf16 v[204:207], v[72:75], a[200:203], v[204:207]
buffer_load_dwordx4 v[68:71], v1, s[16:19], s72 offen offset:1024 sc1
s_waitcnt vmcnt(15)
v_mfma_f32_16x16x32_bf16 v[192:195], v[76:79], a[12:15], v[192:195]
v_mfma_f32_16x16x32_bf16 v[196:199], v[76:79], a[76:79], v[196:199]
buffer_load_dwordx4 v[72:75], v1, s[16:19], s72 offen offset:2048 sc1
v_mfma_f32_16x16x32_bf16 v[200:203], v[76:79], a[140:143], v[200:203]
v_mfma_f32_16x16x32_bf16 v[204:207], v[76:79], a[204:207], v[204:207]
s_waitcnt vmcnt(14)
v_mfma_f32_16x16x32_bf16 v[192:195], v[80:83], a[16:19], v[192:195]
v_mfma_f32_16x16x32_bf16 v[196:199], v[80:83], a[80:83], v[196:199]
buffer_load_dwordx4 v[76:79], v1, s[16:19], s72 offen offset:3072 sc1
v_mfma_f32_16x16x32_bf16 v[200:203], v[80:83], a[144:147], v[200:203]
v_mfma_f32_16x16x32_bf16 v[204:207], v[80:83], a[208:211], v[204:207]
s_waitcnt vmcnt(14)
v_mfma_f32_16x16x32_bf16 v[192:195], v[84:87], a[20:23], v[192:195]
v_mfma_f32_16x16x32_bf16 v[196:199], v[84:87], a[84:87], v[196:199]
s_add_u32 s72, s72, 4096
buffer_load_dwordx4 v[80:83], v1, s[16:19], s72 offen sc1
v_mfma_f32_16x16x32_bf16 v[200:203], v[84:87], a[148:151], v[200:203]
v_mfma_f32_16x16x32_bf16 v[204:207], v[84:87], a[212:215], v[204:207]
s_waitcnt vmcnt(14)
v_mfma_f32_16x16x32_bf16 v[192:195], v[88:91], a[24:27], v[192:195]
v_mfma_f32_16x16x32_bf16 v[196:199], v[88:91], a[88:91], v[196:199]
buffer_load_dwordx4 v[84:87], v1, s[16:19], s72 offen offset:1024 sc1
v_mfma_f32_16x16x32_bf16 v[200:203], v[88:91], a[152:155], v[200:203]
v_mfma_f32_16x16x32_bf16 v[204:207], v[88:91], a[216:219], v[204:207]
s_waitcnt vmcnt(14)
v_mfma_f32_16x16x32_bf16 v[192:195], v[92:95], a[28:31], v[192:195]
v_mfma_f32_16x16x32_bf16 v[196:199], v[92:95], a[92:95], v[196:199]
buffer_load_dwordx4 v[88:91], v1, s[16:19], s72 offen offset:2048 sc1
v_mfma_f32_16x16x32_bf16 v[200:203], v[92:95], a[156:159], v[200:203]
v_mfma_f32_16x16x32_bf16 v[204:207], v[92:95], a[220:223], v[204:207]
s_waitcnt vmcnt(14) lgkmcnt(0)
s_barrier
v_mfma_f32_16x16x32_bf16 v[192:195], v[96:99], a[32:35], v[192:195]
v_mfma_f32_16x16x32_bf16 v[196:199], v[96:99], a[96:99], v[196:199]
buffer_load_dwordx4 v[92:95], v1, s[16:19], s72 offen offset:3072 sc1
v_mfma_f32_16x16x32_bf16 v[200:203], v[96:99], a[160:163], v[200:203]
v_mfma_f32_16x16x32_bf16 v[204:207], v[96:99], a[224:227], v[204:207]
s_waitcnt vmcnt(14)
v_mfma_f32_16x16x32_bf16 v[192:195], v[100:103], a[36:39], v[192:195]
v_mfma_f32_16x16x32_bf16 v[196:199], v[100:103], a[100:103], v[196:199]
s_add_u32 s72, s89, s56
buffer_load_dwordx4 v[96:99], v1, s[16:19], s72 offen sc1
v_mfma_f32_16x16x32_bf16 v[200:203], v[100:103], a[164:167], v[200:203]
v_mfma_f32_16x16x32_bf16 v[204:207], v[100:103], a[228:231], v[204:207]
s_waitcnt vmcnt(14)
v_mfma_f32_16x16x32_bf16 v[192:195], v[104:107], a[40:43], v[192:195]
v_mfma_f32_16x16x32_bf16 v[196:199], v[104:107], a[104:107], v[196:199]
buffer_load_dwordx4 v[100:103], v1, s[16:19], s72 offen offset:1024 sc1
v_mfma_f32_16x16x32_bf16 v[200:203], v[104:107], a[168:171], v[200:203]
v_mfma_f32_16x16x32_bf16 v[204:207], v[104:107], a[232:235], v[204:207]
s_waitcnt vmcnt(14)
v_mfma_f32_16x16x32_bf16 v[192:195], v[108:111], a[44:47], v[192:195]
v_mfma_f32_16x16x32_bf16 v[196:199], v[108:111], a[108:111], v[196:199]
buffer_load_dwordx4 v[104:107], v1, s[16:19], s72 offen offset:2048 sc1
v_mfma_f32_16x16x32_bf16 v[200:203], v[108:111], a[172:175], v[200:203]
v_mfma_f32_16x16x32_bf16 v[204:207], v[108:111], a[236:239], v[204:207]
s_waitcnt vmcnt(14)
v_mfma_f32_16x16x32_bf16 v[192:195], v[112:115], a[48:51], v[192:195]
v_mfma_f32_16x16x32_bf16 v[196:199], v[112:115], a[112:115], v[196:199]
buffer_load_dwordx4 v[108:111], v1, s[16:19], s72 offen offset:3072 sc1
v_mfma_f32_16x16x32_bf16 v[200:203], v[112:115], a[176:179], v[200:203]
v_mfma_f32_16x16x32_bf16 v[204:207], v[112:115], a[240:243], v[204:207]
s_waitcnt vmcnt(14)
v_mfma_f32_16x16x32_bf16 v[192:195], v[116:119], a[52:55], v[192:195]
v_mfma_f32_16x16x32_bf16 v[196:199], v[116:119], a[116:119], v[196:199]
s_add_u32 s72, s72, 4096
buffer_load_dwordx4 v[112:115], v1, s[16:19], s72 offen sc1
v_mfma_f32_16x16x32_bf16 v[200:203], v[116:119], a[180:183], v[200:203]
v_mfma_f32_16x16x32_bf16 v[204:207], v[116:119], a[244:247], v[204:207]
s_waitcnt vmcnt(14)
v_mfma_f32_16x16x32_bf16 v[192:195], v[120:123], a[56:59], v[192:195]
v_mfma_f32_16x16x32_bf16 v[196:199], v[120:123], a[120:123], v[196:199]
buffer_load_dwordx4 v[116:119], v1, s[16:19], s72 offen offset:1024 sc1
v_mfma_f32_16x16x32_bf16 v[200:203], v[120:123], a[184:187], v[200:203]
v_mfma_f32_16x16x32_bf16 v[204:207], v[120:123], a[248:251], v[204:207]
s_waitcnt vmcnt(14)
v_mfma_f32_16x16x32_bf16 v[192:195], v[124:127], a[60:63], v[192:195]
v_mfma_f32_16x16x32_bf16 v[196:199], v[124:127], a[124:127], v[196:199]
buffer_load_dwordx4 v[120:123], v1, s[16:19], s72 offen offset:2048 sc1
v_mfma_f32_16x16x32_bf16 v[200:203], v[124:127], a[188:191], v[200:203]
v_mfma_f32_16x16x32_bf16 v[204:207], v[124:127], a[252:255], v[204:207]
s_waitcnt vmcnt(14)
v_mfma_f32_16x16x32_bf16 v[208:211], v[64:67], a[0:3], 0
v_mfma_f32_16x16x32_bf16 v[212:215], v[64:67], a[64:67], 0
buffer_load_dwordx4 v[124:127], v1, s[16:19], s72 offen offset:3072 sc1
v_mfma_f32_16x16x32_bf16 v[216:219], v[64:67], a[128:131], 0
v_mfma_f32_16x16x32_bf16 v[220:223], v[64:67], a[192:195], 0
ds_write_b128 v8, v[196:199] offset:0
s_waitcnt vmcnt(14)
v_mfma_f32_16x16x32_bf16 v[208:211], v[68:71], a[4:7], v[208:211]
v_mfma_f32_16x16x32_bf16 v[212:215], v[68:71], a[68:71], v[212:215]
s_add_u32 s89, s89, 65536
s_mov_b32 s72, s89
buffer_load_dwordx4 v[64:67], v1, s[16:19], s72 offen sc1
v_mfma_f32_16x16x32_bf16 v[216:219], v[68:71], a[132:135], v[216:219]
ds_write_b128 v9, v[200:203] offset:0
v_mfma_f32_16x16x32_bf16 v[220:223], v[68:71], a[196:199], v[220:223]
s_waitcnt vmcnt(14)
v_mfma_f32_16x16x32_bf16 v[208:211], v[72:75], a[8:11], v[208:211]
v_mfma_f32_16x16x32_bf16 v[212:215], v[72:75], a[72:75], v[212:215]
buffer_load_dwordx4 v[68:71], v1, s[16:19], s72 offen offset:1024 sc1
ds_write_b128 v10, v[204:207] offset:0
v_mfma_f32_16x16x32_bf16 v[216:219], v[72:75], a[136:139], v[216:219]
v_mfma_f32_16x16x32_bf16 v[220:223], v[72:75], a[200:203], v[220:223]
s_waitcnt lgkmcnt(0)
s_barrier
s_waitcnt vmcnt(14)
v_mfma_f32_16x16x32_bf16 v[208:211], v[76:79], a[12:15], v[208:211]
v_mfma_f32_16x16x32_bf16 v[212:215], v[76:79], a[76:79], v[212:215]
buffer_load_dwordx4 v[72:75], v1, s[16:19], s72 offen offset:2048 sc1
v_mfma_f32_16x16x32_bf16 v[216:219], v[76:79], a[140:143], v[216:219]
ds_read_b128 v[224:227], v11 offset:0
v_mfma_f32_16x16x32_bf16 v[220:223], v[76:79], a[204:207], v[220:223]
s_waitcnt vmcnt(14)
v_mfma_f32_16x16x32_bf16 v[208:211], v[80:83], a[16:19], v[208:211]
ds_read_b128 v[228:231], v11 offset:1024
v_mfma_f32_16x16x32_bf16 v[212:215], v[80:83], a[80:83], v[212:215]
buffer_load_dwordx4 v[76:79], v1, s[16:19], s72 offen offset:3072 sc1
v_mfma_f32_16x16x32_bf16 v[216:219], v[80:83], a[144:147], v[216:219]
v_mfma_f32_16x16x32_bf16 v[220:223], v[80:83], a[208:211], v[220:223]
ds_read_b128 v[232:235], v11 offset:2048
s_waitcnt vmcnt(14)
v_mfma_f32_16x16x32_bf16 v[208:211], v[84:87], a[20:23], v[208:211]
v_mfma_f32_16x16x32_bf16 v[212:215], v[84:87], a[84:87], v[212:215]
s_add_u32 s72, s72, 4096
buffer_load_dwordx4 v[80:83], v1, s[16:19], s72 offen sc1
v_add_f32 v128, v192, v12
v_mfma_f32_16x16x32_bf16 v[216:219], v[84:87], a[148:151], v[216:219]
v_mfma_f32_16x16x32_bf16 v[220:223], v[84:87], a[212:215], v[220:223]
s_waitcnt vmcnt(14)
v_mfma_f32_16x16x32_bf16 v[208:211], v[88:91], a[24:27], v[208:211]
v_add_f32 v129, v193, v12
v_mfma_f32_16x16x32_bf16 v[212:215], v[88:91], a[88:91], v[212:215]
buffer_load_dwordx4 v[84:87], v1, s[16:19], s72 offen offset:1024 sc1
v_mfma_f32_16x16x32_bf16 v[216:219], v[88:91], a[152:155], v[216:219]
s_and_b32 exec_lo, 1, s3
s_mov_b32 exec_hi, 0
buffer_load_dword v2, off, s[16:19], s62 sc1
s_mov_b64 exec, -1
v_add_f32 v130, v194, v12
v_mfma_f32_16x16x32_bf16 v[220:223], v[88:91], a[216:219], v[220:223]
s_waitcnt vmcnt(15)
v_mfma_f32_16x16x32_bf16 v[208:211], v[92:95], a[28:31], v[208:211]
v_mfma_f32_16x16x32_bf16 v[212:215], v[92:95], a[92:95], v[212:215]
buffer_load_dwordx4 v[88:91], v1, s[16:19], s72 offen offset:2048 sc1
v_add_f32 v131, v195, v12
v_mfma_f32_16x16x32_bf16 v[216:219], v[92:95], a[156:159], v[216:219]
v_mfma_f32_16x16x32_bf16 v[220:223], v[92:95], a[220:223], v[220:223]
s_waitcnt lgkmcnt(2)
v_add_f32 v128, v128, v224
s_waitcnt vmcnt(15)
v_mfma_f32_16x16x32_bf16 v[208:211], v[96:99], a[32:35], v[208:211]
v_mfma_f32_16x16x32_bf16 v[212:215], v[96:99], a[96:99], v[212:215]
buffer_load_dwordx4 v[92:95], v1, s[16:19], s72 offen offset:3072 sc1
v_mfma_f32_16x16x32_bf16 v[216:219], v[96:99], a[160:163], v[216:219]
v_add_f32 v129, v129, v225
v_mfma_f32_16x16x32_bf16 v[220:223], v[96:99], a[224:227], v[220:223]
s_waitcnt vmcnt(15)
v_mfma_f32_16x16x32_bf16 v[208:211], v[100:103], a[36:39], v[208:211]
v_add_f32 v130, v130, v226
v_mfma_f32_16x16x32_bf16 v[212:215], v[100:103], a[100:103], v[212:215]
s_add_u32 s72, s89, s56
buffer_load_dwordx4 v[96:99], v1, s[16:19], s72 offen sc1
v_mfma_f32_16x16x32_bf16 v[216:219], v[100:103], a[164:167], v[216:219]
v_mfma_f32_16x16x32_bf16 v[220:223], v[100:103], a[228:231], v[220:223]
v_add_f32 v131, v131, v227
s_waitcnt vmcnt(15)
v_mfma_f32_16x16x32_bf16 v[208:211], v[104:107], a[40:43], v[208:211]
v_mfma_f32_16x16x32_bf16 v[212:215], v[104:107], a[104:107], v[212:215]
buffer_load_dwordx4 v[100:103], v1, s[16:19], s72 offen offset:1024 sc1
s_waitcnt lgkmcnt(1)
v_add_f32 v128, v128, v228
v_mfma_f32_16x16x32_bf16 v[216:219], v[104:107], a[168:171], v[216:219]
v_mfma_f32_16x16x32_bf16 v[220:223], v[104:107], a[232:235], v[220:223]
s_waitcnt vmcnt(15)
v_mfma_f32_16x16x32_bf16 v[208:211], v[108:111], a[44:47], v[208:211]
v_add_f32 v129, v129, v229
v_mfma_f32_16x16x32_bf16 v[212:215], v[108:111], a[108:111], v[212:215]
buffer_load_dwordx4 v[104:107], v1, s[16:19], s72 offen offset:2048 sc1
v_mfma_f32_16x16x32_bf16 v[216:219], v[108:111], a[172:175], v[216:219]
v_add_f32 v130, v130, v230
v_mfma_f32_16x16x32_bf16 v[220:223], v[108:111], a[236:239], v[220:223]
s_waitcnt vmcnt(15)
v_mfma_f32_16x16x32_bf16 v[208:211], v[112:115], a[48:51], v[208:211]
v_mfma_f32_16x16x32_bf16 v[212:215], v[112:115], a[112:115], v[212:215]
buffer_load_dwordx4 v[108:111], v1, s[16:19], s72 offen offset:3072 sc1
v_add_f32 v131, v131, v231
v_mfma_f32_16x16x32_bf16 v[216:219], v[112:115], a[176:179], v[216:219]
v_mfma_f32_16x16x32_bf16 v[220:223], v[112:115], a[240:243], v[220:223]
s_waitcnt lgkmcnt(0)
v_add_f32 v128, v128, v232
s_waitcnt vmcnt(15)
v_mfma_f32_16x16x32_bf16 v[208:211], v[116:119], a[52:55], v[208:211]
v_mfma_f32_16x16x32_bf16 v[212:215], v[116:119], a[116:119], v[212:215]
s_add_u32 s72, s72, 4096
buffer_load_dwordx4 v[112:115], v1, s[16:19], s72 offen sc1
v_mfma_f32_16x16x32_bf16 v[216:219], v[116:119], a[180:183], v[216:219]
v_add_f32 v129, v129, v233
v_mfma_f32_16x16x32_bf16 v[220:223], v[116:119], a[244:247], v[220:223]
s_waitcnt vmcnt(15)
v_mfma_f32_16x16x32_bf16 v[208:211], v[120:123], a[56:59], v[208:211]
v_add_f32 v130, v130, v234
v_mfma_f32_16x16x32_bf16 v[212:215], v[120:123], a[120:123], v[212:215]
buffer_load_dwordx4 v[116:119], v1, s[16:19], s72 offen offset:1024 sc1
v_mfma_f32_16x16x32_bf16 v[216:219], v[120:123], a[184:187], v[216:219]
v_mfma_f32_16x16x32_bf16 v[220:223], v[120:123], a[248:251], v[220:223]
v_add_f32 v131, v131, v235
s_waitcnt vmcnt(15)
v_mfma_f32_16x16x32_bf16 v[208:211], v[124:127], a[60:63], v[208:211]
v_mfma_f32_16x16x32_bf16 v[212:215], v[124:127], a[124:127], v[212:215]
buffer_load_dwordx4 v[120:123], v1, s[16:19], s72 offen offset:2048 sc1
v_mfma_f32_16x16x32_bf16 v[216:219], v[124:127], a[188:191], v[216:219]
v_mfma_f32_16x16x32_bf16 v[220:223], v[124:127], a[252:255], v[220:223]
s_waitcnt vmcnt(15)
v_mfma_f32_16x16x32_bf16 v[192:195], v[64:67], a[0:3], 0
v_mfma_f32_16x16x32_bf16 v[196:199], v[64:67], a[64:67], 0
buffer_load_dwordx4 v[124:127], v1, s[16:19], s72 offen offset:3072 sc1
v_mfma_f32_16x16x32_bf16 v[200:203], v[64:67], a[128:131], 0
v_mfma_f32_16x16x32_bf16 v[204:207], v[64:67], a[192:195], 0
ds_write_b128 v8, v[212:215] offset:12288
s_waitcnt vmcnt(15)
v_mfma_f32_16x16x32_bf16 v[192:195], v[68:71], a[4:7], v[192:195]
v_mfma_f32_16x16x32_bf16 v[196:199], v[68:71], a[68:71], v[196:199]
s_add_u32 s89, s89, 65536
s_mov_b32 s72, s89
buffer_load_dwordx4 v[64:67], v1, s[16:19], s72 offen sc1
v_mfma_f32_16x16x32_bf16 v[200:203], v[68:71], a[132:135], v[200:203]
ds_write_b128 v9, v[216:219] offset:12288
v_mfma_f32_16x16x32_bf16 v[204:207], v[68:71], a[196:199], v[204:207]
s_waitcnt vmcnt(15)
v_mfma_f32_16x16x32_bf16 v[192:195], v[72:75], a[8:11], v[192:195]
v_mfma_f32_16x16x32_bf16 v[196:199], v[72:75], a[72:75], v[196:199]
buffer_load_dwordx4 v[68:71], v1, s[16:19], s72 offen offset:1024 sc1
ds_write_b128 v10, v[220:223] offset:12288
v_mfma_f32_16x16x32_bf16 v[200:203], v[72:75], a[136:139], v[200:203]
v_mfma_f32_16x16x32_bf16 v[204:207], v[72:75], a[200:203], v[204:207]
s_waitcnt vmcnt(12)
s_and_b32 exec_lo, 1, s3
s_mov_b32 exec_hi, 0
v_cmp_ne_u32_e32 vcc, s63, v2
s_mov_b64 exec, -1
s_nop 3
s_cbranch_vccz .Lk_z1ok_23
s_mov_b64 exec, 1
s_mov_b32 s66, 32768
.Lk_poll_24:
buffer_load_dword v2, off, s[16:19], s62 sc1
s_waitcnt vmcnt(0)
v_cmp_ne_u32_e32 vcc, s63, v2
s_nop 4
s_cbranch_vccz .Lk_done_25
s_sub_u32 s66, s66, 1
s_cbranch_scc0 .Lk_poll_24
.Lk_done_25:
s_mov_b64 exec, -1
.Lk_z1ok_23:
s_waitcnt lgkmcnt(0)
s_barrier
v_mfma_f32_16x16x32_bf16 v[192:195], v[76:79], a[12:15], v[192:195]
v_mfma_f32_16x16x32_bf16 v[196:199], v[76:79], a[76:79], v[196:199]
buffer_load_dwordx4 v[72:75], v1, s[16:19], s72 offen offset:2048 sc1
v_mfma_f32_16x16x32_bf16 v[200:203], v[76:79], a[140:143], v[200:203]
ds_read_b128 v[224:227], v11 offset:12288
v_mfma_f32_16x16x32_bf16 v[204:207], v[76:79], a[204:207], v[204:207]
v_mfma_f32_16x16x32_bf16 v[192:195], v[80:83], a[16:19], v[192:195]
ds_read_b128 v[228:231], v11 offset:13312
v_mfma_f32_16x16x32_bf16 v[196:199], v[80:83], a[80:83], v[196:199]
buffer_load_dwordx4 v[76:79], v1, s[16:19], s72 offen offset:3072 sc1
v_mfma_f32_16x16x32_bf16 v[200:203], v[80:83], a[144:147], v[200:203]
v_mfma_f32_16x16x32_bf16 v[204:207], v[80:83], a[208:211], v[204:207]
ds_read_b128 v[232:235], v11 offset:14336
v_mfma_f32_16x16x32_bf16 v[192:195], v[84:87], a[20:23], v[192:195]
v_mfma_f32_16x16x32_bf16 v[196:199], v[84:87], a[84:87], v[196:199]
s_add_u32 s72, s72, 4096
buffer_load_dwordx4 v[80:83], v1, s[16:19], s72 offen sc1
v_add_f32 v132, v208, v12
v_mfma_f32_16x16x32_bf16 v[200:203], v[84:87], a[148:151], v[200:203]
v_mfma_f32_16x16x32_bf16 v[204:207], v[84:87], a[212:215], v[204:207]
s_waitcnt vmcnt(14)
v_mfma_f32_16x16x32_bf16 v[192:195], v[88:91], a[24:27], v[192:195]
v_add_f32 v133, v209, v12
v_mfma_f32_16x16x32_bf16 v[196:199], v[88:91], a[88:91], v[196:199]
buffer_load_dwordx4 v[84:87], v1, s[16:19], s72 offen offset:1024 sc1
v_mfma_f32_16x16x32_bf16 v[200:203], v[88:91], a[152:155], v[200:203]
s_add_u32 s67, s96, 12582912
buffer_load_dwordx4 v[236:239], v1, s[16:19], s67 offen sc1
buffer_load_dwordx4 v[240:243], v1, s[16:19], s67 offen offset:1024 sc1
buffer_load_dwordx4 v[244:247], v1, s[16:19], s67 offen offset:2048 sc1
s_add_u32 s67, s97, 12582912
buffer_load_dwordx2 v[248:249], v1, s[16:19], s67 offen sc1
v_add_f32 v134, v210, v12
v_mfma_f32_16x16x32_bf16 v[204:207], v[88:91], a[216:219], v[204:207]
s_waitcnt vmcnt(18)
v_mfma_f32_16x16x32_bf16 v[192:195], v[92:95], a[28:31], v[192:195]
v_mfma_f32_16x16x32_bf16 v[196:199], v[92:95], a[92:95], v[196:199]
buffer_load_dwordx4 v[88:91], v1, s[16:19], s72 offen offset:2048 sc1
v_add_f32 v135, v211, v12
v_mfma_f32_16x16x32_bf16 v[200:203], v[92:95], a[156:159], v[200:203]
v_mfma_f32_16x16x32_bf16 v[204:207], v[92:95], a[220:223], v[204:207]
s_waitcnt lgkmcnt(2)
v_add_f32 v132, v132, v224
s_waitcnt vmcnt(18)
v_mfma_f32_16x16x32_bf16 v[192:195], v[96:99], a[32:35], v[192:195]
v_mfma_f32_16x16x32_bf16 v[196:199], v[96:99], a[96:99], v[196:199]
buffer_load_dwordx4 v[92:95], v1, s[16:19], s72 offen offset:3072 sc1
v_mfma_f32_16x16x32_bf16 v[200:203], v[96:99], a[160:163], v[200:203]
v_add_f32 v133, v133, v225
v_mfma_f32_16x16x32_bf16 v[204:207], v[96:99], a[224:227], v[204:207]
s_waitcnt vmcnt(18)
v_mfma_f32_16x16x32_bf16 v[192:195], v[100:103], a[36:39], v[192:195]
v_add_f32 v134, v134, v226
v_mfma_f32_16x16x32_bf16 v[196:199], v[100:103], a[100:103], v[196:199]
s_add_u32 s72, s89, s56
buffer_load_dwordx4 v[96:99], v1, s[16:19], s72 offen sc1
v_mfma_f32_16x16x32_bf16 v[200:203], v[100:103], a[164:167], v[200:203]
v_mfma_f32_16x16x32_bf16 v[204:207], v[100:103], a[228:231], v[204:207]
v_add_f32 v135, v135, v227
s_waitcnt vmcnt(18)
v_mfma_f32_16x16x32_bf16 v[192:195], v[104:107], a[40:43], v[192:195]
v_mfma_f32_16x16x32_bf16 v[196:199], v[104:107], a[104:107], v[196:199]
buffer_load_dwordx4 v[100:103], v1, s[16:19], s72 offen offset:1024 sc1
s_waitcnt lgkmcnt(1)
v_add_f32 v132, v132, v228
v_mfma_f32_16x16x32_bf16 v[200:203], v[104:107], a[168:171], v[200:203]
v_mfma_f32_16x16x32_bf16 v[204:207], v[104:107], a[232:235], v[204:207]
s_waitcnt vmcnt(18)
v_mfma_f32_16x16x32_bf16 v[192:195], v[108:111], a[44:47], v[192:195]
v_add_f32 v133, v133, v229
v_mfma_f32_16x16x32_bf16 v[196:199], v[108:111], a[108:111], v[196:199]
buffer_load_dwordx4 v[104:107], v1, s[16:19], s72 offen offset:2048 sc1
v_mfma_f32_16x16x32_bf16 v[200:203], v[108:111], a[172:175], v[200:203]
v_add_f32 v134, v134, v230
v_mfma_f32_16x16x32_bf16 v[204:207], v[108:111], a[236:239], v[204:207]
s_waitcnt vmcnt(18)
v_mfma_f32_16x16x32_bf16 v[192:195], v[112:115], a[48:51], v[192:195]
v_mfma_f32_16x16x32_bf16 v[196:199], v[112:115], a[112:115], v[196:199]
buffer_load_dwordx4 v[108:111], v1, s[16:19], s72 offen offset:3072 sc1
v_add_f32 v135, v135, v231
v_mfma_f32_16x16x32_bf16 v[200:203], v[112:115], a[176:179], v[200:203]
v_mfma_f32_16x16x32_bf16 v[204:207], v[112:115], a[240:243], v[204:207]
s_waitcnt lgkmcnt(0)
v_add_f32 v132, v132, v232
s_waitcnt vmcnt(18)
v_mfma_f32_16x16x32_bf16 v[192:195], v[116:119], a[52:55], v[192:195]
v_mfma_f32_16x16x32_bf16 v[196:199], v[116:119], a[116:119], v[196:199]
s_add_u32 s72, s72, 4096
buffer_load_dwordx4 v[112:115], v1, s[16:19], s72 offen sc1
v_mfma_f32_16x16x32_bf16 v[200:203], v[116:119], a[180:183], v[200:203]
v_add_f32 v133, v133, v233
v_mfma_f32_16x16x32_bf16 v[204:207], v[116:119], a[244:247], v[204:207]
s_waitcnt vmcnt(18)
v_mfma_f32_16x16x32_bf16 v[192:195], v[120:123], a[56:59], v[192:195]
v_add_f32 v134, v134, v234
v_mfma_f32_16x16x32_bf16 v[196:199], v[120:123], a[120:123], v[196:199]
buffer_load_dwordx4 v[116:119], v1, s[16:19], s72 offen offset:1024 sc1
v_mfma_f32_16x16x32_bf16 v[200:203], v[120:123], a[184:187], v[200:203]
v_mfma_f32_16x16x32_bf16 v[204:207], v[120:123], a[248:251], v[204:207]
v_add_f32 v135, v135, v235
s_waitcnt vmcnt(18)
v_mfma_f32_16x16x32_bf16 v[192:195], v[124:127], a[60:63], v[192:195]
v_mfma_f32_16x16x32_bf16 v[196:199], v[124:127], a[124:127], v[196:199]
buffer_load_dwordx4 v[120:123], v1, s[16:19], s72 offen offset:2048 sc1
v_mfma_f32_16x16x32_bf16 v[200:203], v[124:127], a[188:191], v[200:203]
v_mfma_f32_16x16x32_bf16 v[204:207], v[124:127], a[252:255], v[204:207]
s_waitcnt vmcnt(18)
v_mfma_f32_16x16x32_bf16 v[208:211], v[64:67], a[0:3], 0
v_mfma_f32_16x16x32_bf16 v[212:215], v[64:67], a[64:67], 0
buffer_load_dwordx4 v[124:127], v1, s[16:19], s72 offen offset:3072 sc1
v_mfma_f32_16x16x32_bf16 v[216:219], v[64:67], a[128:131], 0
v_mfma_f32_16x16x32_bf16 v[220:223], v[64:67], a[192:195], 0
ds_write_b128 v8, v[196:199] offset:0
s_waitcnt vmcnt(18)
v_mfma_f32_16x16x32_bf16 v[208:211], v[68:71], a[4:7], v[208:211]
v_mfma_f32_16x16x32_bf16 v[212:215], v[68:71], a[68:71], v[212:215]
s_add_u32 s89, s89, 65536
s_mov_b32 s72, s89
buffer_load_dwordx4 v[64:67], v1, s[16:19], s72 offen sc1
v_mfma_f32_16x16x32_bf16 v[216:219], v[68:71], a[132:135], v[216:219]
ds_write_b128 v9, v[200:203] offset:0
v_mfma_f32_16x16x32_bf16 v[220:223], v[68:71], a[196:199], v[220:223]
s_waitcnt vmcnt(18)
v_mfma_f32_16x16x32_bf16 v[208:211], v[72:75], a[8:11], v[208:211]
v_mfma_f32_16x16x32_bf16 v[212:215], v[72:75], a[72:75], v[212:215]
buffer_load_dwordx4 v[68:71], v1, s[16:19], s72 offen offset:1024 sc1
ds_write_b128 v10, v[204:207] offset:0
v_mfma_f32_16x16x32_bf16 v[216:219], v[72:75], a[136:139], v[216:219]
v_mfma_f32_16x16x32_bf16 v[220:223], v[72:75], a[200:203], v[220:223]
s_waitcnt lgkmcnt(0)
s_barrier
s_waitcnt vmcnt(18)
v_mfma_f32_16x16x32_bf16 v[208:211], v[76:79], a[12:15], v[208:211]
v_mfma_f32_16x16x32_bf16 v[212:215], v[76:79], a[76:79], v[212:215]
buffer_load_dwordx4 v[72:75], v1, s[16:19], s72 offen offset:2048 sc1
v_mfma_f32_16x16x32_bf16 v[216:219], v[76:79], a[140:143], v[216:219]
ds_read_b128 v[224:227], v11 offset:0
v_mfma_f32_16x16x32_bf16 v[220:223], v[76:79], a[204:207], v[220:223]
s_waitcnt vmcnt(18)
v_mfma_f32_16x16x32_bf16 v[208:211], v[80:83], a[16:19], v[208:211]
ds_read_b128 v[228:231], v11 offset:1024
v_mfma_f32_16x16x32_bf16 v[212:215], v[80:83], a[80:83], v[212:215]
buffer_load_dwordx4 v[76:79], v1, s[16:19], s72 offen offset:3072 sc1
v_mfma_f32_16x16x32_bf16 v[216:219], v[80:83], a[144:147], v[216:219]
v_mfma_f32_16x16x32_bf16 v[220:223], v[80:83], a[208:211], v[220:223]
ds_read_b128 v[232:235], v11 offset:2048
s_waitcnt vmcnt(18)
v_mfma_f32_16x16x32_bf16 v[208:211], v[84:87], a[20:23], v[208:211]
v_mfma_f32_16x16x32_bf16 v[212:215], v[84:87], a[84:87], v[212:215]
s_add_u32 s72, s72, 4096
buffer_load_dwordx4 v[80:83], v1, s[16:19], s72 offen sc1
v_add_f32 v136, v192, v12
v_mfma_f32_16x16x32_bf16 v[216:219], v[84:87], a[148:151], v[216:219]
v_mfma_f32_16x16x32_bf16 v[220:223], v[84:87], a[212:215], v[220:223]
s_waitcnt vmcnt(14)
v_mfma_f32_16x16x32_bf16 v[208:211], v[88:91], a[24:27], v[208:211]
v_add_f32 v137, v193, v12
v_mfma_f32_16x16x32_bf16 v[212:215], v[88:91], a[88:91], v[212:215]
buffer_load_dwordx4 v[84:87], v1, s[16:19], s72 offen offset:1024 sc1
v_mfma_f32_16x16x32_bf16 v[216:219], v[88:91], a[152:155], v[216:219]
v_add_f32 v138, v194, v12
v_mfma_f32_16x16x32_bf16 v[220:223], v[88:91], a[216:219], v[220:223]
s_waitcnt vmcnt(14)
v_mfma_f32_16x16x32_bf16 v[208:211], v[92:95], a[28:31], v[208:211]
v_mfma_f32_16x16x32_bf16 v[212:215], v[92:95], a[92:95], v[212:215]
buffer_load_dwordx4 v[88:91], v1, s[16:19], s72 offen offset:2048 sc1
v_add_f32 v139, v195, v12
v_mfma_f32_16x16x32_bf16 v[216:219], v[92:95], a[156:159], v[216:219]
v_mfma_f32_16x16x32_bf16 v[220:223], v[92:95], a[220:223], v[220:223]
s_waitcnt lgkmcnt(2)
v_add_f32 v136, v136, v224
s_waitcnt vmcnt(14)
v_mfma_f32_16x16x32_bf16 v[208:211], v[96:99], a[32:35], v[208:211]
v_mfma_f32_16x16x32_bf16 v[212:215], v[96:99], a[96:99], v[212:215]
buffer_load_dwordx4 v[92:95], v1, s[16:19], s72 offen offset:3072 sc1
v_mfma_f32_16x16x32_bf16 v[216:219], v[96:99], a[160:163], v[216:219]
v_add_f32 v137, v137, v225
v_mfma_f32_16x16x32_bf16 v[220:223], v[96:99], a[224:227], v[220:223]
s_waitcnt vmcnt(14)
v_mfma_f32_16x16x32_bf16 v[208:211], v[100:103], a[36:39], v[208:211]
v_add_f32 v138, v138, v226
v_mfma_f32_16x16x32_bf16 v[212:215], v[100:103], a[100:103], v[212:215]
s_add_u32 s72, s89, s56
buffer_load_dwordx4 v[96:99], v1, s[16:19], s72 offen sc1
v_mfma_f32_16x16x32_bf16 v[216:219], v[100:103], a[164:167], v[216:219]
v_mfma_f32_16x16x32_bf16 v[220:223], v[100:103], a[228:231], v[220:223]
v_add_f32 v139, v139, v227
s_waitcnt vmcnt(14)
v_mfma_f32_16x16x32_bf16 v[208:211], v[104:107], a[40:43], v[208:211]
v_mfma_f32_16x16x32_bf16 v[212:215], v[104:107], a[104:107], v[212:215]
buffer_load_dwordx4 v[100:103], v1, s[16:19], s72 offen offset:1024 sc1
s_waitcnt lgkmcnt(1)
v_add_f32 v136, v136, v228
v_mfma_f32_16x16x32_bf16 v[216:219], v[104:107], a[168:171], v[216:219]
v_mfma_f32_16x16x32_bf16 v[220:223], v[104:107], a[232:235], v[220:223]
s_waitcnt vmcnt(14)
v_mfma_f32_16x16x32_bf16 v[208:211], v[108:111], a[44:47], v[208:211]
v_add_f32 v137, v137, v229
v_mfma_f32_16x16x32_bf16 v[212:215], v[108:111], a[108:111], v[212:215]
buffer_load_dwordx4 v[104:107], v1, s[16:19], s72 offen offset:2048 sc1
v_mfma_f32_16x16x32_bf16 v[216:219], v[108:111], a[172:175], v[216:219]
v_add_f32 v138, v138, v230
v_mfma_f32_16x16x32_bf16 v[220:223], v[108:111], a[236:239], v[220:223]
s_waitcnt vmcnt(14)
v_mfma_f32_16x16x32_bf16 v[208:211], v[112:115], a[48:51], v[208:211]
v_mfma_f32_16x16x32_bf16 v[212:215], v[112:115], a[112:115], v[212:215]
buffer_load_dwordx4 v[108:111], v1, s[16:19], s72 offen offset:3072 sc1
v_add_f32 v139, v139, v231
v_mfma_f32_16x16x32_bf16 v[216:219], v[112:115], a[176:179], v[216:219]
v_mfma_f32_16x16x32_bf16 v[220:223], v[112:115], a[240:243], v[220:223]
s_waitcnt lgkmcnt(0)
v_add_f32 v136, v136, v232
s_waitcnt vmcnt(14)
v_mfma_f32_16x16x32_bf16 v[208:211], v[116:119], a[52:55], v[208:211]
v_mfma_f32_16x16x32_bf16 v[212:215], v[116:119], a[116:119], v[212:215]
s_add_u32 s72, s72, 4096
buffer_load_dwordx4 v[112:115], v1, s[16:19], s72 offen sc1
v_mfma_f32_16x16x32_bf16 v[216:219], v[116:119], a[180:183], v[216:219]
v_add_f32 v137, v137, v233
v_mfma_f32_16x16x32_bf16 v[220:223], v[116:119], a[244:247], v[220:223]
s_waitcnt vmcnt(14)
v_mfma_f32_16x16x32_bf16 v[208:211], v[120:123], a[56:59], v[208:211]
v_add_f32 v138, v138, v234
v_mfma_f32_16x16x32_bf16 v[212:215], v[120:123], a[120:123], v[212:215]
buffer_load_dwordx4 v[116:119], v1, s[16:19], s72 offen offset:1024 sc1
v_mfma_f32_16x16x32_bf16 v[216:219], v[120:123], a[184:187], v[216:219]
v_mfma_f32_16x16x32_bf16 v[220:223], v[120:123], a[248:251], v[220:223]
v_add_f32 v139, v139, v235
s_waitcnt vmcnt(14)
v_mfma_f32_16x16x32_bf16 v[208:211], v[124:127], a[60:63], v[208:211]
v_mfma_f32_16x16x32_bf16 v[212:215], v[124:127], a[124:127], v[212:215]
buffer_load_dwordx4 v[120:123], v1, s[16:19], s72 offen offset:2048 sc1
v_mfma_f32_16x16x32_bf16 v[216:219], v[124:127], a[188:191], v[216:219]
v_mfma_f32_16x16x32_bf16 v[220:223], v[124:127], a[252:255], v[220:223]
s_waitcnt vmcnt(14)
v_mfma_f32_16x16x32_bf16 v[192:195], v[64:67], a[0:3], 0
v_mfma_f32_16x16x32_bf16 v[196:199], v[64:67], a[64:67], 0
buffer_load_dwordx4 v[124:127], v1, s[16:19], s72 offen offset:3072 sc1
v_mfma_f32_16x16x32_bf16 v[200:203], v[64:67], a[128:131], 0
v_mfma_f32_16x16x32_bf16 v[204:207], v[64:67], a[192:195], 0
ds_write_b128 v8, v[212:215] offset:12288
s_waitcnt vmcnt(14)
v_mfma_f32_16x16x32_bf16 v[192:195], v[68:71], a[4:7], v[192:195]
v_mfma_f32_16x16x32_bf16 v[196:199], v[68:71], a[68:71], v[196:199]
s_add_u32 s89, s89, 65536
s_mov_b32 s72, s89
buffer_load_dwordx4 v[64:67], v1, s[16:19], s72 offen sc1
ds_write_b128 v9, v[216:219] offset:12288
v_mfma_f32_16x16x32_bf16 v[200:203], v[68:71], a[132:135], v[200:203]
ds_write_b128 v10, v[220:223] offset:12288
v_mfma_f32_16x16x32_bf16 v[204:207], v[68:71], a[196:199], v[204:207]
s_waitcnt lgkmcnt(0)
s_barrier
s_waitcnt vmcnt(14)
v_mfma_f32_16x16x32_bf16 v[192:195], v[72:75], a[8:11], v[192:195]
v_mfma_f32_16x16x32_bf16 v[196:199], v[72:75], a[72:75], v[196:199]
buffer_load_dwordx4 v[68:71], v1, s[16:19], s72 offen offset:1024 sc1
ds_read_b128 v[224:227], v11 offset:12288
v_mfma_f32_16x16x32_bf16 v[200:203], v[72:75], a[136:139], v[200:203]
ds_read_b128 v[228:231], v11 offset:13312
v_mfma_f32_16x16x32_bf16 v[204:207], v[72:75], a[200:203], v[204:207]
ds_read_b128 v[232:235], v11 offset:14336
s_waitcnt vmcnt(14)
v_mfma_f32_16x16x32_bf16 v[192:195], v[76:79], a[12:15], v[192:195]
v_mfma_f32_16x16x32_bf16 v[196:199], v[76:79], a[76:79], v[196:199]
buffer_load_dwordx4 v[72:75], v1, s[16:19], s72 offen offset:2048 sc1
v_add_f32 v140, v208, v12
v_mfma_f32_16x16x32_bf16 v[200:203], v[76:79], a[140:143], v[200:203]
v_add_f32 v141, v209, v12
v_mfma_f32_16x16x32_bf16 v[204:207], v[76:79], a[204:207], v[204:207]
v_add_f32 v142, v210, v12
s_waitcnt vmcnt(14)
v_mfma_f32_16x16x32_bf16 v[192:195], v[80:83], a[16:19], v[192:195]
v_add_f32 v143, v211, v12
v_mfma_f32_16x16x32_bf16 v[196:199], v[80:83], a[80:83], v[196:199]
buffer_load_dwordx4 v[76:79], v1, s[16:19], s72 offen offset:3072 sc1
v_mfma_f32_16x16x32_bf16 v[200:203], v[80:83], a[144:147], v[200:203]
s_waitcnt lgkmcnt(2)
v_add_f32 v140, v140, v224
v_mfma_f32_16x16x32_bf16 v[204:207], v[80:83], a[208:211], v[204:207]
v_add_f32 v141, v141, v225
s_waitcnt vmcnt(14)
v_mfma_f32_16x16x32_bf16 v[192:195], v[84:87], a[20:23], v[192:195]
v_add_f32 v142, v142, v226
v_mfma_f32_16x16x32_bf16 v[196:199], v[84:87], a[84:87], v[196:199]
s_add_u32 s72, s72, 4096
buffer_load_dwordx4 v[80:83], v1, s[16:19], s72 offen sc1
v_mfma_f32_16x16x32_bf16 v[200:203], v[84:87], a[148:151], v[200:203]
v_add_f32 v143, v143, v227
v_mfma_f32_16x16x32_bf16 v[204:207], v[84:87], a[212:215], v[204:207]
s_waitcnt lgkmcnt(1)
v_add_f32 v140, v140, v228
s_waitcnt vmcnt(14)
v_mfma_f32_16x16x32_bf16 v[192:195], v[88:91], a[24:27], v[192:195]
v_add_f32 v141, v141, v229
v_mfma_f32_16x16x32_bf16 v[196:199], v[88:91], a[88:91], v[196:199]
buffer_load_dwordx4 v[84:87], v1, s[16:19], s72 offen offset:1024 sc1
v_add_f32 v142, v142, v230
v_mfma_f32_16x16x32_bf16 v[200:203], v[88:91], a[152:155], v[200:203]
v_mfma_f32_16x16x32_bf16 v[204:207], v[88:91], a[216:219], v[204:207]
v_add_f32 v143, v143, v231
s_waitcnt vmcnt(14)
v_mfma_f32_16x16x32_bf16 v[192:195], v[92:95], a[28:31], v[192:195]
s_waitcnt lgkmcnt(0)
v_add_f32 v140, v140, v232
v_mfma_f32_16x16x32_bf16 v[196:199], v[92:95], a[92:95], v[196:199]
buffer_load_dwordx4 v[88:91], v1, s[16:19], s72 offen offset:2048 sc1
v_add_f32 v141, v141, v233
v_mfma_f32_16x16x32_bf16 v[200:203], v[92:95], a[156:159], v[200:203]
v_mfma_f32_16x16x32_bf16 v[204:207], v[92:95], a[220:223], v[204:207]
v_add_f32 v142, v142, v234
s_waitcnt vmcnt(14)
v_mfma_f32_16x16x32_bf16 v[192:195], v[96:99], a[32:35], v[192:195]
v_add_f32 v143, v143, v235
v_mfma_f32_16x16x32_bf16 v[196:199], v[96:99], a[96:99], v[196:199]
buffer_load_dwordx4 v[92:95], v1, s[16:19], s72 offen offset:3072 sc1
v_mfma_f32_16x16x32_bf16 v[200:203], v[96:99], a[160:163], v[200:203]
v_lshlrev_b32 v18, 16, v236
v_mfma_f32_16x16x32_bf16 v[204:207], v[96:99], a[224:227], v[204:207]
s_waitcnt vmcnt(14)
v_mfma_f32_16x16x32_bf16 v[192:195], v[100:103], a[36:39], v[192:195]
v_and_b32 v19, 0xffff0000, v236
v_mfma_f32_16x16x32_bf16 v[196:199], v[100:103], a[100:103], v[196:199]
s_add_u32 s72, s89, s56
buffer_load_dwordx4 v[96:99], v1, s[16:19], s72 offen sc1
v_mul_f32 v128, v128, v18
v_mfma_f32_16x16x32_bf16 v[200:203], v[100:103], a[164:167], v[200:203]
v_mul_f32 v129, v129, v19
v_mfma_f32_16x16x32_bf16 v[204:207], v[100:103], a[228:231], v[204:207]
s_waitcnt vmcnt(14)
v_mfma_f32_16x16x32_bf16 v[192:195], v[104:107], a[40:43], v[192:195]
v_lshlrev_b32 v18, 16, v237
v_mfma_f32_16x16x32_bf16 v[196:199], v[104:107], a[104:107], v[196:199]
buffer_load_dwordx4 v[100:103], v1, s[16:19], s72 offen offset:1024 sc1
v_and_b32 v19, 0xffff0000, v237
v_mfma_f32_16x16x32_bf16 v[200:203], v[104:107], a[168:171], v[200:203]
v_mul_f32 v130, v130, v18
v_mfma_f32_16x16x32_bf16 v[204:207], v[104:107], a[232:235], v[204:207]
v_mul_f32 v131, v131, v19
s_waitcnt vmcnt(14)
v_mfma_f32_16x16x32_bf16 v[192:195], v[108:111], a[44:47], v[192:195]
v_mfma_f32_16x16x32_bf16 v[196:199], v[108:111], a[108:111], v[196:199]
buffer_load_dwordx4 v[104:107], v1, s[16:19], s72 offen offset:2048 sc1
v_cvt_pk_bf16_f32 v156, v128, v129
v_mfma_f32_16x16x32_bf16 v[200:203], v[108:111], a[172:175], v[200:203]
v_cvt_pk_bf16_f32 v157, v130, v131
v_mfma_f32_16x16x32_bf16 v[204:207], v[108:111], a[236:239], v[204:207]
s_waitcnt vmcnt(14)
v_mfma_f32_16x16x32_bf16 v[192:195], v[112:115], a[48:51], v[192:195]
v_mfma_f32_16x16x32_bf16 v[196:199], v[112:115], a[112:115], v[196:199]
buffer_load_dwordx4 v[108:111], v1, s[16:19], s72 offen offset:3072 sc1
v_lshlrev_b32 v18, 16, v238
v_mfma_f32_16x16x32_bf16 v[200:203], v[112:115], a[176:179], v[200:203]
v_and_b32 v19, 0xffff0000, v238
v_mfma_f32_16x16x32_bf16 v[204:207], v[112:115], a[240:243], v[204:207]
v_mul_f32 v132, v132, v18
s_waitcnt vmcnt(14)
v_mfma_f32_16x16x32_bf16 v[192:195], v[116:119], a[52:55], v[192:195]
v_mul_f32 v133, v133, v19
v_mfma_f32_16x16x32_bf16 v[196:199], v[116:119], a[116:119], v[196:199]
s_add_u32 s72, s72, 4096
buffer_load_dwordx4 v[112:115], v1, s[16:19], s72 offen sc1
v_mfma_f32_16x16x32_bf16 v[200:203], v[116:119], a[180:183], v[200:203]
v_lshlrev_b32 v18, 16, v239
v_mfma_f32_16x16x32_bf16 v[204:207], v[116:119], a[244:247], v[204:207]
v_and_b32 v19, 0xffff0000, v239
s_waitcnt vmcnt(14)
v_mfma_f32_16x16x32_bf16 v[192:195], v[120:123], a[56:59], v[192:195]
v_mul_f32 v134, v134, v18
v_mfma_f32_16x16x32_bf16 v[196:199], v[120:123], a[120:123], v[196:199]
buffer_load_dwordx4 v[116:119], v1, s[16:19], s72 offen offset:1024 sc1
v_mfma_f32_16x16x32_bf16 v[200:203], v[120:123], a[184:187], v[200:203]
v_mul_f32 v135, v135, v19
v_mfma_f32_16x16x32_bf16 v[204:207], v[120:123], a[248:251], v[204:207]
v_cvt_pk_bf16_f32 v158, v132, v133
s_waitcnt vmcnt(14)
v_mfma_f32_16x16x32_bf16 v[192:195], v[124:127], a[60:63], v[192:195]
v_cvt_pk_bf16_f32 v159, v134, v135
v_mfma_f32_16x16x32_bf16 v[196:199], v[124:127], a[124:127], v[196:199]
buffer_load_dwordx4 v[120:123], v1, s[16:19], s72 offen offset:2048 sc1
v_mfma_f32_16x16x32_bf16 v[200:203], v[124:127], a[188:191], v[200:203]
v_mfma_f32_16x16x32_bf16 v[204:207], v[124:127], a[252:255], v[204:207]
s_waitcnt vmcnt(14)
v_mfma_f32_16x16x32_bf16 v[208:211], v[64:67], a[0:3], 0
v_mfma_f32_16x16x32_bf16 v[212:215], v[64:67], a[64:67], 0
buffer_load_dwordx4 v[124:127], v1, s[16:19], s72 offen offset:3072 sc1
v_mfma_f32_16x16x32_bf16 v[216:219], v[64:67], a[128:131], 0
v_mfma_f32_16x16x32_bf16 v[220:223], v[64:67], a[192:195], 0
ds_write_b128 v8, v[196:199] offset:0
s_waitcnt vmcnt(14)
v_mfma_f32_16x16x32_bf16 v[208:211], v[68:71], a[4:7], v[208:211]
v_mfma_f32_16x16x32_bf16 v[212:215], v[68:71], a[68:71], v[212:215]
s_mov_b32 s72, s90
buffer_load_dwordx4 v[64:67], v1, s[16:19], s72 offen sc1
ds_write_b128 v9, v[200:203] offset:0
v_mfma_f32_16x16x32_bf16 v[216:219], v[68:71], a[132:135], v[216:219]
ds_write_b128 v10, v[204:207] offset:0
v_mfma_f32_16x16x32_bf16 v[220:223], v[68:71], a[196:199], v[220:223]
s_waitcnt lgkmcnt(0)
s_barrier
s_waitcnt vmcnt(14)
v_mfma_f32_16x16x32_bf16 v[208:211], v[72:75], a[8:11], v[208:211]
v_mfma_f32_16x16x32_bf16 v[212:215], v[72:75], a[72:75], v[212:215]
buffer_load_dwordx4 v[68:71], v1, s[16:19], s72 offen offset:1024 sc1
ds_read_b128 v[224:227], v11 offset:0
v_mfma_f32_16x16x32_bf16 v[216:219], v[72:75], a[136:139], v[216:219]
ds_read_b128 v[228:231], v11 offset:1024
v_mfma_f32_16x16x32_bf16 v[220:223], v[72:75], a[200:203], v[220:223]
ds_read_b128 v[232:235], v11 offset:2048
s_waitcnt vmcnt(14)
v_mfma_f32_16x16x32_bf16 v[208:211], v[76:79], a[12:15], v[208:211]
v_mfma_f32_16x16x32_bf16 v[212:215], v[76:79], a[76:79], v[212:215]
buffer_load_dwordx4 v[72:75], v1, s[16:19], s72 offen offset:2048 sc1
v_add_f32 v144, v192, v12
v_mfma_f32_16x16x32_bf16 v[216:219], v[76:79], a[140:143], v[216:219]
v_add_f32 v145, v193, v12
v_mfma_f32_16x16x32_bf16 v[220:223], v[76:79], a[204:207], v[220:223]
v_add_f32 v146, v194, v12
s_waitcnt vmcnt(14)
v_mfma_f32_16x16x32_bf16 v[208:211], v[80:83], a[16:19], v[208:211]
v_add_f32 v147, v195, v12
v_mfma_f32_16x16x32_bf16 v[212:215], v[80:83], a[80:83], v[212:215]
buffer_load_dwordx4 v[76:79], v1, s[16:19], s72 offen offset:3072 sc1
v_mfma_f32_16x16x32_bf16 v[216:219], v[80:83], a[144:147], v[216:219]
s_waitcnt lgkmcnt(2)
v_add_f32 v144, v144, v224
v_mfma_f32_16x16x32_bf16 v[220:223], v[80:83], a[208:211], v[220:223]
v_add_f32 v145, v145, v225
s_waitcnt vmcnt(14)
v_mfma_f32_16x16x32_bf16 v[208:211], v[84:87], a[20:23], v[208:211]
v_add_f32 v146, v146, v226
v_mfma_f32_16x16x32_bf16 v[212:215], v[84:87], a[84:87], v[212:215]
s_add_u32 s72, s72, 4096
buffer_load_dwordx4 v[80:83], v1, s[16:19], s72 offen sc1
v_mfma_f32_16x16x32_bf16 v[216:219], v[84:87], a[148:151], v[216:219]
v_add_f32 v147, v147, v227
v_mfma_f32_16x16x32_bf16 v[220:223], v[84:87], a[212:215], v[220:223]
s_waitcnt lgkmcnt(1)
v_add_f32 v144, v144, v228
s_waitcnt vmcnt(14)
v_mfma_f32_16x16x32_bf16 v[208:211], v[88:91], a[24:27], v[208:211]
v_add_f32 v145, v145, v229
v_mfma_f32_16x16x32_bf16 v[212:215], v[88:91], a[88:91], v[212:215]
buffer_load_dwordx4 v[84:87], v1, s[16:19], s72 offen offset:1024 sc1
v_add_f32 v146, v146, v230
v_mfma_f32_16x16x32_bf16 v[216:219], v[88:91], a[152:155], v[216:219]
v_mfma_f32_16x16x32_bf16 v[220:223], v[88:91], a[216:219], v[220:223]
v_add_f32 v147, v147, v231
s_waitcnt vmcnt(14)
v_mfma_f32_16x16x32_bf16 v[208:211], v[92:95], a[28:31], v[208:211]
s_waitcnt lgkmcnt(0)
v_add_f32 v144, v144, v232
v_mfma_f32_16x16x32_bf16 v[212:215], v[92:95], a[92:95], v[212:215]
buffer_load_dwordx4 v[88:91], v1, s[16:19], s72 offen offset:2048 sc1
v_add_f32 v145, v145, v233
v_mfma_f32_16x16x32_bf16 v[216:219], v[92:95], a[156:159], v[216:219]
v_mfma_f32_16x16x32_bf16 v[220:223], v[92:95], a[220:223], v[220:223]
v_add_f32 v146, v146, v234
s_waitcnt vmcnt(14)
v_mfma_f32_16x16x32_bf16 v[208:211], v[96:99], a[32:35], v[208:211]
v_add_f32 v147, v147, v235
v_mfma_f32_16x16x32_bf16 v[212:215], v[96:99], a[96:99], v[212:215]
buffer_load_dwordx4 v[92:95], v1, s[16:19], s72 offen offset:3072 sc1
v_mfma_f32_16x16x32_bf16 v[216:219], v[96:99], a[160:163], v[216:219]
v_lshlrev_b32 v18, 16, v240
v_mfma_f32_16x16x32_bf16 v[220:223], v[96:99], a[224:227], v[220:223]
s_waitcnt vmcnt(14)
v_mfma_f32_16x16x32_bf16 v[208:211], v[100:103], a[36:39], v[208:211]
v_and_b32 v19, 0xffff0000, v240
v_mfma_f32_16x16x32_bf16 v[212:215], v[100:103], a[100:103], v[212:215]
s_add_u32 s72, s90, s56
buffer_load_dwordx4 v[96:99], v1, s[16:19], s72 offen sc1
v_mul_f32 v136, v136, v18
v_mfma_f32_16x16x32_bf16 v[216:219], v[100:103], a[164:167], v[216:219]
v_mul_f32 v137, v137, v19
v_mfma_f32_16x16x32_bf16 v[220:223], v[100:103], a[228:231], v[220:223]
s_waitcnt vmcnt(14)
v_mfma_f32_16x16x32_bf16 v[208:211], v[104:107], a[40:43], v[208:211]
v_lshlrev_b32 v18, 16, v241
v_mfma_f32_16x16x32_bf16 v[212:215], v[104:107], a[104:107], v[212:215]
buffer_load_dwordx4 v[100:103], v1, s[16:19], s72 offen offset:1024 sc1
v_and_b32 v19, 0xffff0000, v241
v_mfma_f32_16x16x32_bf16 v[216:219], v[104:107], a[168:171], v[216:219]
v_mul_f32 v138, v138, v18
v_mfma_f32_16x16x32_bf16 v[220:223], v[104:107], a[232:235], v[220:223]
v_mul_f32 v139, v139, v19
s_waitcnt vmcnt(14)
v_mfma_f32_16x16x32_bf16 v[208:211], v[108:111], a[44:47], v[208:211]
v_mfma_f32_16x16x32_bf16 v[212:215], v[108:111], a[108:111], v[212:215]
buffer_load_dwordx4 v[104:107], v1, s[16:19], s72 offen offset:2048 sc1
v_cvt_pk_bf16_f32 v160, v136, v137
v_mfma_f32_16x16x32_bf16 v[216:219], v[108:111], a[172:175], v[216:219]
v_cvt_pk_bf16_f32 v161, v138, v139
v_mfma_f32_16x16x32_bf16 v[220:223], v[108:111], a[236:239], v[220:223]
s_waitcnt vmcnt(14)
v_mfma_f32_16x16x32_bf16 v[208:211], v[112:115], a[48:51], v[208:211]
v_mfma_f32_16x16x32_bf16 v[212:215], v[112:115], a[112:115], v[212:215]
buffer_load_dwordx4 v[108:111], v1, s[16:19], s72 offen offset:3072 sc1
v_lshlrev_b32 v18, 16, v242
v_mfma_f32_16x16x32_bf16 v[216:219], v[112:115], a[176:179], v[216:219]
v_and_b32 v19, 0xffff0000, v242
v_mfma_f32_16x16x32_bf16 v[220:223], v[112:115], a[240:243], v[220:223]
v_mul_f32 v140, v140, v18
s_waitcnt vmcnt(14)
v_mfma_f32_16x16x32_bf16 v[208:211], v[116:119], a[52:55], v[208:211]
v_mul_f32 v141, v141, v19
v_mfma_f32_16x16x32_bf16 v[212:215], v[116:119], a[116:119], v[212:215]
s_add_u32 s72, s72, 4096
buffer_load_dwordx4 v[112:115], v1, s[16:19], s72 offen sc1
v_mfma_f32_16x16x32_bf16 v[216:219], v[116:119], a[180:183], v[216:219]
v_lshlrev_b32 v18, 16, v243
v_mfma_f32_16x16x32_bf16 v[220:223], v[116:119], a[244:247], v[220:223]
v_and_b32 v19, 0xffff0000, v243
s_waitcnt vmcnt(14)
v_mfma_f32_16x16x32_bf16 v[208:211], v[120:123], a[56:59], v[208:211]
v_mul_f32 v142, v142, v18
v_mfma_f32_16x16x32_bf16 v[212:215], v[120:123], a[120:123], v[212:215]
buffer_load_dwordx4 v[116:119], v1, s[16:19], s72 offen offset:1024 sc1
v_mfma_f32_16x16x32_bf16 v[216:219], v[120:123], a[184:187], v[216:219]
v_mul_f32 v143, v143, v19
v_mfma_f32_16x16x32_bf16 v[220:223], v[120:123], a[248:251], v[220:223]
v_cvt_pk_bf16_f32 v162, v140, v141
s_waitcnt vmcnt(14)
v_mfma_f32_16x16x32_bf16 v[208:211], v[124:127], a[60:63], v[208:211]
v_cvt_pk_bf16_f32 v163, v142, v143
v_mfma_f32_16x16x32_bf16 v[212:215], v[124:127], a[124:127], v[212:215]
buffer_load_dwordx4 v[120:123], v1, s[16:19], s72 offen offset:2048 sc1
v_mfma_f32_16x16x32_bf16 v[216:219], v[124:127], a[188:191], v[216:219]
v_mfma_f32_16x16x32_bf16 v[220:223], v[124:127], a[252:255], v[220:223]
s_waitcnt vmcnt(14)
v_mfma_f32_16x16x32_bf16 v[192:195], v[64:67], a[0:3], 0
v_mfma_f32_16x16x32_bf16 v[196:199], v[64:67], a[64:67], 0
buffer_load_dwordx4 v[124:127], v1, s[16:19], s72 offen offset:3072 sc1
v_mfma_f32_16x16x32_bf16 v[200:203], v[64:67], a[128:131], 0
v_mfma_f32_16x16x32_bf16 v[204:207], v[64:67], a[192:195], 0
ds_write_b128 v8, v[212:215] offset:12288
s_waitcnt vmcnt(14)
v_mfma_f32_16x16x32_bf16 v[192:195], v[68:71], a[4:7], v[192:195]
ds_write_b128 v9, v[216:219] offset:12288
v_mfma_f32_16x16x32_bf16 v[196:199], v[68:71], a[68:71], v[196:199]
ds_write_b128 v10, v[220:223] offset:12288
v_mfma_f32_16x16x32_bf16 v[200:203], v[68:71], a[132:135], v[200:203]
s_add_u32 s67, s96, 16777216
buffer_store_dwordx4 v[156:159], v1, s[16:19], s67 offen sc1
s_waitcnt lgkmcnt(0)
s_barrier
ds_read_b128 v[224:227], v11 offset:12288
v_mfma_f32_16x16x32_bf16 v[204:207], v[68:71], a[196:199], v[204:207]
ds_read_b128 v[228:231], v11 offset:13312
s_waitcnt vmcnt(14)
v_mfma_f32_16x16x32_bf16 v[192:195], v[72:75], a[8:11], v[192:195]
ds_read_b128 v[232:235], v11 offset:14336
v_add_f32 v148, v208, v12
v_mfma_f32_16x16x32_bf16 v[196:199], v[72:75], a[72:75], v[196:199]
v_add_f32 v149, v209, v12
v_mfma_f32_16x16x32_bf16 v[200:203], v[72:75], a[136:139], v[200:203]
s_add_u32 s67, s96, 16777216
buffer_store_dwordx4 v[160:163], v1, s[16:19], s67 offen offset:1024 sc1
v_add_f32 v150, v210, v12
v_add_f32 v151, v211, v12
v_mfma_f32_16x16x32_bf16 v[204:207], v[72:75], a[200:203], v[204:207]
s_mul_i32 s6, s49, 14
s_lshl_b32 s7, s6, 10
s_add_u32 m0, s7, 0
s_mul_i32 s7, s86, 28
s_add_u32 s6, s6, s7
s_add_u32 s6, s6, 0
s_mul_i32 s6, s6, 7168
s_cmp_ge_u32 s6, 802816
s_cselect_b32 s7, 802816, 0
s_sub_u32 s6, s6, s7
buffer_load_dwordx4 v32, s[36:39], s6 offen lds
s_waitcnt lgkmcnt(2)
v_add_f32 v148, v148, v224
s_waitcnt vmcnt(15)
v_mfma_f32_16x16x32_bf16 v[192:195], v[76:79], a[12:15], v[192:195]
v_add_f32 v149, v149, v225
v_mfma_f32_16x16x32_bf16 v[196:199], v[76:79], a[76:79], v[196:199]
v_add_f32 v150, v150, v226
v_add_f32 v151, v151, v227
v_mfma_f32_16x16x32_bf16 v[200:203], v[76:79], a[140:143], v[200:203]
s_waitcnt lgkmcnt(1)
v_add_f32 v148, v148, v228
v_mfma_f32_16x16x32_bf16 v[204:207], v[76:79], a[204:207], v[204:207]
s_add_u32 m0, m0, 1024
s_add_u32 s6, s6, 7168
s_cmp_ge_u32 s6, 802816
s_cselect_b32 s7, 802816, 0
s_sub_u32 s6, s6, s7
buffer_load_dwordx4 v32, s[36:39], s6 offen lds
v_add_f32 v149, v149, v229
v_add_f32 v150, v150, v230
s_waitcnt vmcnt(15)
v_mfma_f32_16x16x32_bf16 v[192:195], v[80:83], a[16:19], v[192:195]
v_add_f32 v151, v151, v231
v_mfma_f32_16x16x32_bf16 v[196:199], v[80:83], a[80:83], v[196:199]
s_waitcnt lgkmcnt(0)
v_add_f32 v148, v148, v232
v_add_f32 v149, v149, v233
v_mfma_f32_16x16x32_bf16 v[200:203], v[80:83], a[144:147], v[200:203]
v_add_f32 v150, v150, v234
v_mfma_f32_16x16x32_bf16 v[204:207], v[80:83], a[208:211], v[204:207]
s_add_u32 m0, m0, 1024
s_add_u32 s6, s6, 7168
s_cmp_ge_u32 s6, 802816
s_cselect_b32 s7, 802816, 0
s_sub_u32 s6, s6, s7
buffer_load_dwordx4 v32, s[36:39], s6 offen lds
v_add_f32 v151, v151, v235
s_waitcnt vmcnt(15)
v_mfma_f32_16x16x32_bf16 v[192:195], v[84:87], a[20:23], v[192:195]
v_lshlrev_b32 v18, 16, v244
v_mfma_f32_16x16x32_bf16 v[196:199], v[84:87], a[84:87], v[196:199]
v_and_b32 v19, 0xffff0000, v244
v_mfma_f32_16x16x32_bf16 v[200:203], v[84:87], a[148:151], v[200:203]
v_mul_f32 v144, v144, v18
v_mul_f32 v145, v145, v19
v_mfma_f32_16x16x32_bf16 v[204:207], v[84:87], a[212:215], v[204:207]
s_add_u32 m0, m0, 1024
s_add_u32 s6, s6, 7168
s_cmp_ge_u32 s6, 802816
s_cselect_b32 s7, 802816, 0
s_sub_u32 s6, s6, s7
buffer_load_dwordx4 v32, s[36:39], s6 offen lds
v_lshlrev_b32 v18, 16, v245
s_waitcnt vmcnt(15)
v_mfma_f32_16x16x32_bf16 v[192:195], v[88:91], a[24:27], v[192:195]
v_and_b32 v19, 0xffff0000, v245
v_mul_f32 v146, v146, v18
v_mfma_f32_16x16x32_bf16 v[196:199], v[88:91], a[88:91], v[196:199]
v_mul_f32 v147, v147, v19
v_mfma_f32_16x16x32_bf16 v[200:203], v[88:91], a[152:155], v[200:203]
v_cvt_pk_bf16_f32 v164, v144, v145
v_cvt_pk_bf16_f32 v165, v146, v147
v_mfma_f32_16x16x32_bf16 v[204:207], v[88:91], a[216:219], v[204:207]
s_add_u32 m0, m0, 1024
s_add_u32 s6, s6, 7168
s_cmp_ge_u32 s6, 802816
s_cselect_b32 s7, 802816, 0
s_sub_u32 s6, s6, s7
buffer_load_dwordx4 v32, s[36:39], s6 offen lds
s_waitcnt vmcnt(15)
v_mfma_f32_16x16x32_bf16 v[192:195], v[92:95], a[28:31], v[192:195]
v_lshlrev_b32 v18, 16, v246
v_mfma_f32_16x16x32_bf16 v[196:199], v[92:95], a[92:95], v[196:199]
v_and_b32 v19, 0xffff0000, v246
v_mul_f32 v148, v148, v18
v_mfma_f32_16x16x32_bf16 v[200:203], v[92:95], a[156:159], v[200:203]
v_mul_f32 v149, v149, v19
v_mfma_f32_16x16x32_bf16 v[204:207], v[92:95], a[220:223], v[204:207]
s_add_u32 m0, m0, 1024
s_add_u32 s6, s6, 7168
s_cmp_ge_u32 s6, 802816
s_cselect_b32 s7, 802816, 0
s_sub_u32 s6, s6, s7
buffer_load_dwordx4 v32, s[36:39], s6 offen lds
v_lshlrev_b32 v18, 16, v247
v_and_b32 v19, 0xffff0000, v247
s_waitcnt vmcnt(15)
v_mfma_f32_16x16x32_bf16 v[192:195], v[96:99], a[32:35], v[192:195]
v_mul_f32 v150, v150, v18
v_mfma_f32_16x16x32_bf16 v[196:199], v[96:99], a[96:99], v[196:199]
v_mul_f32 v151, v151, v19
v_cvt_pk_bf16_f32 v166, v148, v149
v_mfma_f32_16x16x32_bf16 v[200:203], v[96:99], a[160:163], v[200:203]
v_cvt_pk_bf16_f32 v167, v150, v151
v_mfma_f32_16x16x32_bf16 v[204:207], v[96:99], a[224:227], v[204:207]
s_add_u32 m0, m0, 1024
s_add_u32 s6, s6, 7168
s_cmp_ge_u32 s6, 802816
s_cselect_b32 s7, 802816, 0
s_sub_u32 s6, s6, s7
buffer_load_dwordx4 v32, s[36:39], s6 offen lds
s_add_u32 s67, s96, 16777216
buffer_store_dwordx4 v[164:167], v1, s[16:19], s67 offen offset:2048 sc1
s_waitcnt vmcnt(16)
v_mfma_f32_16x16x32_bf16 v[192:195], v[100:103], a[36:39], v[192:195]
v_mfma_f32_16x16x32_bf16 v[196:199], v[100:103], a[100:103], v[196:199]
v_mfma_f32_16x16x32_bf16 v[200:203], v[100:103], a[164:167], v[200:203]
v_mfma_f32_16x16x32_bf16 v[204:207], v[100:103], a[228:231], v[204:207]
s_add_u32 m0, m0, 1024
s_add_u32 s6, s6, 7168
s_cmp_ge_u32 s6, 802816
s_cselect_b32 s7, 802816, 0
s_sub_u32 s6, s6, s7
buffer_load_dwordx4 v32, s[36:39], s6 offen lds
s_waitcnt vmcnt(16)
v_mfma_f32_16x16x32_bf16 v[192:195], v[104:107], a[40:43], v[192:195]
v_mfma_f32_16x16x32_bf16 v[196:199], v[104:107], a[104:107], v[196:199]
v_mfma_f32_16x16x32_bf16 v[200:203], v[104:107], a[168:171], v[200:203]
v_mfma_f32_16x16x32_bf16 v[204:207], v[104:107], a[232:235], v[204:207]
s_add_u32 m0, m0, 1024
s_add_u32 s6, s6, 7168
s_cmp_ge_u32 s6, 802816
s_cselect_b32 s7, 802816, 0
s_sub_u32 s6, s6, s7
buffer_load_dwordx4 v32, s[36:39], s6 offen lds
s_waitcnt vmcnt(16)
v_mfma_f32_16x16x32_bf16 v[192:195], v[108:111], a[44:47], v[192:195]
v_mfma_f32_16x16x32_bf16 v[196:199], v[108:111], a[108:111], v[196:199]
v_mfma_f32_16x16x32_bf16 v[200:203], v[108:111], a[172:175], v[200:203]
v_mfma_f32_16x16x32_bf16 v[204:207], v[108:111], a[236:239], v[204:207]
s_add_u32 m0, m0, 1024
s_add_u32 s6, s6, 7168
s_cmp_ge_u32 s6, 802816
s_cselect_b32 s7, 802816, 0
s_sub_u32 s6, s6, s7
buffer_load_dwordx4 v32, s[36:39], s6 offen lds
s_waitcnt vmcnt(16)
v_mfma_f32_16x16x32_bf16 v[192:195], v[112:115], a[48:51], v[192:195]
v_mfma_f32_16x16x32_bf16 v[196:199], v[112:115], a[112:115], v[196:199]
v_mfma_f32_16x16x32_bf16 v[200:203], v[112:115], a[176:179], v[200:203]
v_mfma_f32_16x16x32_bf16 v[204:207], v[112:115], a[240:243], v[204:207]
s_add_u32 m0, m0, 1024
s_add_u32 s6, s6, 7168
s_cmp_ge_u32 s6, 802816
s_cselect_b32 s7, 802816, 0
s_sub_u32 s6, s6, s7
buffer_load_dwordx4 v32, s[36:39], s6 offen lds
s_waitcnt vmcnt(16)
v_mfma_f32_16x16x32_bf16 v[192:195], v[116:119], a[52:55], v[192:195]
v_mfma_f32_16x16x32_bf16 v[196:199], v[116:119], a[116:119], v[196:199]
v_mfma_f32_16x16x32_bf16 v[200:203], v[116:119], a[180:183], v[200:203]
v_mfma_f32_16x16x32_bf16 v[204:207], v[116:119], a[244:247], v[204:207]
s_add_u32 m0, m0, 1024
s_add_u32 s6, s6, 7168
s_cmp_ge_u32 s6, 802816
s_cselect_b32 s7, 802816, 0
s_sub_u32 s6, s6, s7
buffer_load_dwordx4 v32, s[36:39], s6 offen lds
s_waitcnt vmcnt(16)
v_mfma_f32_16x16x32_bf16 v[192:195], v[120:123], a[56:59], v[192:195]
v_mfma_f32_16x16x32_bf16 v[196:199], v[120:123], a[120:123], v[196:199]
v_mfma_f32_16x16x32_bf16 v[200:203], v[120:123], a[184:187], v[200:203]
v_mfma_f32_16x16x32_bf16 v[204:207], v[120:123], a[248:251], v[204:207]
s_add_u32 m0, m0, 1024
s_add_u32 s6, s6, 7168
s_cmp_ge_u32 s6, 802816
s_cselect_b32 s7, 802816, 0
s_sub_u32 s6, s6, s7
buffer_load_dwordx4 v32, s[36:39], s6 offen lds
s_waitcnt vmcnt(16)
v_mfma_f32_16x16x32_bf16 v[192:195], v[124:127], a[60:63], v[192:195]
v_mfma_f32_16x16x32_bf16 v[196:199], v[124:127], a[124:127], v[196:199]
v_mfma_f32_16x16x32_bf16 v[200:203], v[124:127], a[188:191], v[200:203]
v_mfma_f32_16x16x32_bf16 v[204:207], v[124:127], a[252:255], v[204:207]
s_add_u32 m0, m0, 1024
s_add_u32 s6, s6, 7168
s_cmp_ge_u32 s6, 802816
s_cselect_b32 s7, 802816, 0
s_sub_u32 s6, s6, s7
buffer_load_dwordx4 v32, s[36:39], s6 offen lds
ds_write_b128 v8, v[196:199] offset:0
ds_write_b128 v9, v[200:203] offset:0
ds_write_b128 v10, v[204:207] offset:0
s_waitcnt vmcnt(7)
s_waitcnt lgkmcnt(0)
s_barrier
s_add_u32 s67, s60, 1081344
s_and_b32 exec_lo, 1, s3
s_mov_b32 exec_hi, 0
buffer_atomic_add v3, off, s[16:19], s67
s_mov_b64 exec, -1
ds_read_b128 v[224:227], v11 offset:0
ds_read_b128 v[228:231], v11 offset:1024
ds_read_b128 v[232:235], v11 offset:2048
v_add_f32 v152, v192, v12
v_add_f32 v153, v193, v12
v_add_f32 v154, v194, v12
v_add_f32 v155, v195, v12
s_waitcnt lgkmcnt(2)
v_add_f32 v152, v152, v224
v_add_f32 v153, v153, v225
v_add_f32 v154, v154, v226
v_add_f32 v155, v155, v227
s_waitcnt lgkmcnt(1)
v_add_f32 v152, v152, v228
v_add_f32 v153, v153, v229
v_add_f32 v154, v154, v230
v_add_f32 v155, v155, v231
s_waitcnt lgkmcnt(0)
v_add_f32 v152, v152, v232
v_add_f32 v153, v153, v233
v_add_f32 v154, v154, v234
v_add_f32 v155, v155, v235
v_lshlrev_b32 v18, 16, v248
v_and_b32 v19, 0xffff0000, v248
v_mul_f32 v152, v152, v18
v_mul_f32 v153, v153, v19
v_lshlrev_b32 v18, 16, v249
v_and_b32 v19, 0xffff0000, v249
v_mul_f32 v154, v154, v18
v_mul_f32 v155, v155, v19
v_cvt_pk_bf16_f32 v168, v152, v153
v_cvt_pk_bf16_f32 v169, v154, v155
s_add_u32 s67, s97, 16777216
buffer_store_dwordx2 v[168:169], v1, s[16:19], s67 offen sc1
s_mul_i32 s6, s49, 14
s_lshl_b32 s7, s6, 10
s_add_u32 m0, s7, 57344
s_mul_i32 s7, s86, 28
s_add_u32 s6, s6, s7
s_add_u32 s6, s6, 56
s_mul_i32 s6, s6, 7168
s_cmp_ge_u32 s6, 802816
s_cselect_b32 s7, 802816, 0
s_sub_u32 s6, s6, s7
buffer_load_dwordx4 v32, s[36:39], s6 offen lds
v_bfe_u32 v12, v0, 2, 4
v_bfe_u32 v13, v0, 3, 2
v_lshlrev_b32 v12, 7, v12
s_add_u32 m0, m0, 1024
s_add_u32 s6, s6, 7168
s_cmp_ge_u32 s6, 802816
s_cselect_b32 s7, 802816, 0
s_sub_u32 s6, s6, s7
buffer_load_dwordx4 v32, s[36:39], s6 offen lds
v_lshl_add_u32 v12, v13, 5, v12
v_and_b32 v13, 3, v0
v_lshl_add_u32 v4, v13, 3, v12
s_add_u32 m0, m0, 1024
s_add_u32 s6, s6, 7168
s_cmp_ge_u32 s6, 802816
s_cselect_b32 s7, 802816, 0
s_sub_u32 s6, s6, s7
buffer_load_dwordx4 v32, s[36:39], s6 offen lds
v_xor_b32 v5, 32, v4
v_xor_b32 v6, 64, v4
v_xor_b32 v7, 96, v4
s_add_u32 m0, m0, 1024
s_add_u32 s6, s6, 7168
s_cmp_ge_u32 s6, 802816
s_cselect_b32 s7, 802816, 0
s_sub_u32 s6, s6, s7
buffer_load_dwordx4 v32, s[36:39], s6 offen lds
v_add_u32 v8, 57344, v4
v_add_u32 v9, 57344, v5
v_add_u32 v10, 57344, v6
s_add_u32 m0, m0, 1024
s_add_u32 s6, s6, 7168
s_cmp_ge_u32 s6, 802816
s_cselect_b32 s7, 802816, 0
s_sub_u32 s6, s6, s7
buffer_load_dwordx4 v32, s[36:39], s6 offen lds
v_add_u32 v11, 57344, v7
s_lshr_b32 s69, s54, 2
s_add_u32 m0, m0, 1024
s_add_u32 s6, s6, 7168
s_cmp_ge_u32 s6, 802816
s_cselect_b32 s7, 802816, 0
s_sub_u32 s6, s6, s7
buffer_load_dwordx4 v32, s[36:39], s6 offen lds
s_lshl_b32 s69, s69, 7
s_add_u32 s70, s69, 1081344
s_add_u32 s69, s69, 1097728
s_add_u32 m0, m0, 1024
s_add_u32 s6, s6, 7168
s_cmp_ge_u32 s6, 802816
s_cselect_b32 s7, 802816, 0
s_sub_u32 s6, s6, s7
buffer_load_dwordx4 v32, s[36:39], s6 offen lds
s_mul_i32 s71, s54, 28672
s_add_u32 s71, s71, 16777216
s_add_u32 s68, s86, 0
s_add_u32 m0, m0, 1024
s_add_u32 s6, s6, 7168
s_cmp_ge_u32 s6, 802816
s_cselect_b32 s7, 802816, 0
s_sub_u32 s6, s6, s7
buffer_load_dwordx4 v32, s[36:39], s6 offen lds
s_and_b32 s68, s68, 3
s_mul_i32 s68, s68, 7168
s_add_u32 s89, s71, s68
s_add_u32 m0, m0, 1024
s_add_u32 s6, s6, 7168
s_cmp_ge_u32 s6, 802816
s_cselect_b32 s7, 802816, 0
s_sub_u32 s6, s6, s7
buffer_load_dwordx4 v32, s[36:39], s6 offen lds
s_add_u32 s93, s89, 4096
s_add_u32 s68, s86, 1
s_and_b32 s68, s68, 3
s_add_u32 m0, m0, 1024
s_add_u32 s6, s6, 7168
s_cmp_ge_u32 s6, 802816
s_cselect_b32 s7, 802816, 0
s_sub_u32 s6, s6, s7
buffer_load_dwordx4 v32, s[36:39], s6 offen lds
s_mul_i32 s68, s68, 7168
s_add_u32 s90, s71, s68
s_add_u32 m0, m0, 1024
s_add_u32 s6, s6, 7168
s_cmp_ge_u32 s6, 802816
s_cselect_b32 s7, 802816, 0
s_sub_u32 s6, s6, s7
buffer_load_dwordx4 v32, s[36:39], s6 offen lds
s_add_u32 s94, s90, 4096
s_add_u32 s68, s86, 2
s_and_b32 s68, s68, 3
s_add_u32 m0, m0, 1024
s_add_u32 s6, s6, 7168
s_cmp_ge_u32 s6, 802816
s_cselect_b32 s7, 802816, 0
s_sub_u32 s6, s6, s7
buffer_load_dwordx4 v32, s[36:39], s6 offen lds
s_mul_i32 s68, s68, 7168
s_add_u32 s91, s71, s68
s_add_u32 s95, s91, 4096
s_add_u32 m0, m0, 1024
s_add_u32 s6, s6, 7168
s_cmp_ge_u32 s6, 802816
s_cselect_b32 s7, 802816, 0
s_sub_u32 s6, s6, s7
buffer_load_dwordx4 v32, s[36:39], s6 offen lds
s_add_u32 s68, s86, 3
s_and_b32 s68, s68, 3
s_mul_i32 s68, s68, 7168
s_add_u32 m0, m0, 1024
s_add_u32 s6, s6, 7168
s_cmp_ge_u32 s6, 802816
s_cselect_b32 s7, 802816, 0
s_sub_u32 s6, s6, s7
buffer_load_dwordx4 v32, s[36:39], s6 offen lds
s_add_u32 s92, s71, s68
s_add_u32 s96, s92, 4096
s_cmp_ge_u32 s51, 14
s_cbranch_scc0 .Lk_p5
s_waitcnt vmcnt(14)
s_barrier
s_add_u32 s67, s60, 1097728
s_and_b32 exec_lo, 1, s3
s_mov_b32 exec_hi, 0
buffer_atomic_add v3, off, s[16:19], s67
s_mov_b64 exec, -1
s_waitcnt vmcnt(0)
s_branch .Lk_fin
.Lk_p5:
s_and_b32 exec_lo, 1, s3
s_mov_b32 exec_hi, 0
s_mov_b32 s66, 32768
.Lk_poll_26:
buffer_load_dword v2, off, s[16:19], s70 sc1
s_waitcnt vmcnt(0)
v_cmp_ne_u32_e32 vcc, 8, v2
s_nop 4
s_cbranch_vccz .Lk_done_27
s_sub_u32 s66, s66, 1
s_cbranch_scc0 .Lk_poll_26
.Lk_done_27:
s_mov_b64 exec, -1
s_barrier
s_add_u32 s67, s60, 1097728
s_and_b32 exec_lo, 1, s3
s_mov_b32 exec_hi, 0
buffer_atomic_add v3, off, s[16:19], s67
s_mov_b64 exec, -1
buffer_load_dwordx4 v[64:67], v1, s[16:19], s89 offen sc1
buffer_load_dwordx4 v[68:71], v1, s[16:19], s89 offen offset:1024 sc1
buffer_load_dwordx4 v[72:75], v1, s[16:19], s89 offen offset:2048 sc1
buffer_load_dwordx4 v[76:79], v1, s[16:19], s93 offen sc1
buffer_load_dwordx4 v[80:83], v1, s[16:19], s93 offen offset:1024 sc1
buffer_load_dwordx4 v[84:87], v1, s[16:19], s93 offen offset:2048 sc1
buffer_load_dwordx4 v[88:91], v1, s[16:19], s90 offen sc1
buffer_load_dwordx4 v[92:95], v1, s[16:19], s90 offen offset:1024 sc1
ds_read_b64_tr_b16 v[176:177], v4 offset:0
ds_read_b64_tr_b16 v[178:179], v4 offset:2048
ds_read_b64_tr_b16 v[180:181], v5 offset:0
ds_read_b64_tr_b16 v[182:183], v5 offset:2048
ds_read_b64_tr_b16 v[184:185], v6 offset:0
ds_read_b64_tr_b16 v[186:187], v6 offset:2048
ds_read_b64_tr_b16 v[188:189], v7 offset:0
ds_read_b64_tr_b16 v[190:191], v7 offset:2048
ds_read_b64_tr_b16 v[192:193], v4 offset:4096
ds_read_b64_tr_b16 v[194:195], v4 offset:6144
ds_read_b64_tr_b16 v[196:197], v5 offset:4096
ds_read_b64_tr_b16 v[198:199], v5 offset:6144
ds_read_b64_tr_b16 v[200:201], v6 offset:4096
ds_read_b64_tr_b16 v[202:203], v6 offset:6144
ds_read_b64_tr_b16 v[204:205], v7 offset:4096
ds_read_b64_tr_b16 v[206:207], v7 offset:6144
ds_read_b64_tr_b16 v[208:209], v4 offset:8192
ds_read_b64_tr_b16 v[210:211], v4 offset:10240
ds_read_b64_tr_b16 v[212:213], v5 offset:8192
ds_read_b64_tr_b16 v[214:215], v5 offset:10240
ds_read_b64_tr_b16 v[216:217], v6 offset:8192
ds_read_b64_tr_b16 v[218:219], v6 offset:10240
ds_read_b64_tr_b16 v[220:221], v7 offset:8192
ds_read_b64_tr_b16 v[222:223], v7 offset:10240
s_lshl_b32 s67, s49, 11
s_add_u32 s67, s67, 139264
v_and_b32 v12, 15, v0
v_lshrrev_b32 v13, 5, v0
v_and_b32 v14, 7, v0
v_xor_b32 v13, v13, v14
v_lshlrev_b32 v12, 7, v12
v_bfe_u32 v14, v0, 4, 1
v_lshl_add_u32 v12, v14, 3, v12
v_add_u32 v12, s67, v12
v_xor_b32 v14, 0, v13
v_lshl_add_u32 v16, v14, 4, v12
v_xor_b32 v14, 2, v13
v_lshl_add_u32 v17, v14, 4, v12
v_xor_b32 v14, 4, v13
v_lshl_add_u32 v18, v14, 4, v12
v_xor_b32 v14, 6, v13
v_lshl_add_u32 v19, v14, 4, v12
v_lshrrev_b32 v12, 3, v0
v_and_b32 v13, 7, v0
v_xor_b32 v14, v12, v13
v_lshlrev_b32 v15, 7, v12
v_lshl_add_u32 v14, v14, 4, v15
v_add_u32 v14, s67, v14
v_mul_u32_u24 v12, 896, v12
v_lshl_add_u32 v15, v13, 4, v12
s_mul_i32 s71, s54, 14336
s_lshl_b32 s72, s65, 7
s_add_u32 s71, s71, s72
s_add_u32 s72, s71, 7168
v_lshlrev_b32 v240, 16, v24
v_and_b32 v241, 0xffff0000, v24
v_lshlrev_b32 v242, 16, v25
v_and_b32 v243, 0xffff0000, v25
v_lshlrev_b32 v244, 16, v26
v_and_b32 v245, 0xffff0000, v26
v_lshlrev_b32 v246, 16, v27
v_and_b32 v247, 0xffff0000, v27
v_lshlrev_b32 v248, 16, v28
v_and_b32 v249, 0xffff0000, v28
v_lshlrev_b32 v250, 16, v29
v_and_b32 v251, 0xffff0000, v29
v_lshlrev_b32 v252, 16, v30
v_and_b32 v253, 0xffff0000, v30
v_lshlrev_b32 v254, 16, v31
v_and_b32 v255, 0xffff0000, v31
ds_read_b64_tr_b16 v[224:225], v4 offset:16384
ds_read_b64_tr_b16 v[226:227], v4 offset:18432
ds_read_b64_tr_b16 v[228:229], v5 offset:16384
ds_read_b64_tr_b16 v[230:231], v5 offset:18432
ds_read_b64_tr_b16 v[232:233], v6 offset:16384
ds_read_b64_tr_b16 v[234:235], v6 offset:18432
ds_read_b64_tr_b16 v[236:237], v7 offset:16384
ds_read_b64_tr_b16 v[238:239], v7 offset:18432
s_waitcnt vmcnt(7) lgkmcnt(14)
v_mfma_f32_16x16x32_bf16 v[48:51], v[176:179], v[64:67], 0
buffer_load_dwordx4 v[96:99], v1, s[16:19], s90 offen offset:2048 sc1
v_mfma_f32_16x16x32_bf16 v[52:55], v[180:183], v[64:67], 0
v_mfma_f32_16x16x32_bf16 v[56:59], v[184:187], v[64:67], 0
v_mfma_f32_16x16x32_bf16 v[60:63], v[188:191], v[64:67], 0
ds_read_b64_tr_b16 v[176:177], v4 offset:20480
ds_read_b64_tr_b16 v[178:179], v4 offset:22528
ds_read_b64_tr_b16 v[180:181], v5 offset:20480
ds_read_b64_tr_b16 v[182:183], v5 offset:22528
ds_read_b64_tr_b16 v[184:185], v6 offset:20480
ds_read_b64_tr_b16 v[186:187], v6 offset:22528
ds_read_b64_tr_b16 v[188:189], v7 offset:20480
ds_read_b64_tr_b16 v[190:191], v7 offset:22528
s_waitcnt vmcnt(7)
v_mfma_f32_16x16x32_bf16 v[48:51], v[192:195], v[68:71], v[48:51]
buffer_load_dwordx4 v[100:103], v1, s[16:19], s94 offen sc1
v_mfma_f32_16x16x32_bf16 v[52:55], v[196:199], v[68:71], v[52:55]
v_mfma_f32_16x16x32_bf16 v[56:59], v[200:203], v[68:71], v[56:59]
v_mfma_f32_16x16x32_bf16 v[60:63], v[204:207], v[68:71], v[60:63]
ds_read_b64_tr_b16 v[192:193], v4 offset:24576
ds_read_b64_tr_b16 v[194:195], v4 offset:26624
ds_read_b64_tr_b16 v[196:197], v5 offset:24576
ds_read_b64_tr_b16 v[198:199], v5 offset:26624
ds_read_b64_tr_b16 v[200:201], v6 offset:24576
ds_read_b64_tr_b16 v[202:203], v6 offset:26624
ds_read_b64_tr_b16 v[204:205], v7 offset:24576
ds_read_b64_tr_b16 v[206:207], v7 offset:26624
s_waitcnt vmcnt(7) lgkmcnt(14)
v_mfma_f32_16x16x32_bf16 v[48:51], v[208:211], v[72:75], v[48:51]
buffer_load_dwordx4 v[104:107], v1, s[16:19], s94 offen offset:1024 sc1
v_mfma_f32_16x16x32_bf16 v[52:55], v[212:215], v[72:75], v[52:55]
v_mfma_f32_16x16x32_bf16 v[56:59], v[216:219], v[72:75], v[56:59]
v_mfma_f32_16x16x32_bf16 v[60:63], v[220:223], v[72:75], v[60:63]
ds_read_b64_tr_b16 v[208:209], v4 offset:28672
ds_read_b64_tr_b16 v[210:211], v4 offset:30720
ds_read_b64_tr_b16 v[212:213], v5 offset:28672
ds_read_b64_tr_b16 v[214:215], v5 offset:30720
ds_read_b64_tr_b16 v[216:217], v6 offset:28672
ds_read_b64_tr_b16 v[218:219], v6 offset:30720
ds_read_b64_tr_b16 v[220:221], v7 offset:28672
ds_read_b64_tr_b16 v[222:223], v7 offset:30720
s_waitcnt vmcnt(7)
v_mfma_f32_16x16x32_bf16 v[48:51], v[224:227], v[76:79], v[48:51]
buffer_load_dwordx4 v[108:111], v1, s[16:19], s94 offen offset:2048 sc1
v_mfma_f32_16x16x32_bf16 v[52:55], v[228:231], v[76:79], v[52:55]
v_mfma_f32_16x16x32_bf16 v[56:59], v[232:235], v[76:79], v[56:59]
v_mfma_f32_16x16x32_bf16 v[60:63], v[236:239], v[76:79], v[60:63]
ds_read_b64_tr_b16 v[224:225], v4 offset:32768
ds_read_b64_tr_b16 v[226:227], v4 offset:34816
ds_read_b64_tr_b16 v[228:229], v5 offset:32768
ds_read_b64_tr_b16 v[230:231], v5 offset:34816
ds_read_b64_tr_b16 v[232:233], v6 offset:32768
ds_read_b64_tr_b16 v[234:235], v6 offset:34816
ds_read_b64_tr_b16 v[236:237], v7 offset:32768
ds_read_b64_tr_b16 v[238:239], v7 offset:34816
s_waitcnt vmcnt(7) lgkmcnt(14)
v_mfma_f32_16x16x32_bf16 v[48:51], v[176:179], v[80:83], v[48:51]
buffer_load_dwordx4 v[112:115], v1, s[16:19], s91 offen sc1
v_mfma_f32_16x16x32_bf16 v[52:55], v[180:183], v[80:83], v[52:55]
v_mfma_f32_16x16x32_bf16 v[56:59], v[184:187], v[80:83], v[56:59]
v_mfma_f32_16x16x32_bf16 v[60:63], v[188:191], v[80:83], v[60:63]
ds_read_b64_tr_b16 v[176:177], v4 offset:36864
ds_read_b64_tr_b16 v[178:179], v4 offset:38912
ds_read_b64_tr_b16 v[180:181], v5 offset:36864
ds_read_b64_tr_b16 v[182:183], v5 offset:38912
ds_read_b64_tr_b16 v[184:185], v6 offset:36864
ds_read_b64_tr_b16 v[186:187], v6 offset:38912
ds_read_b64_tr_b16 v[188:189], v7 offset:36864
ds_read_b64_tr_b16 v[190:191], v7 offset:38912
s_waitcnt vmcnt(7)
v_mfma_f32_16x16x32_bf16 v[48:51], v[192:195], v[84:87], v[48:51]
buffer_load_dwordx4 v[116:119], v1, s[16:19], s91 offen offset:1024 sc1
v_mfma_f32_16x16x32_bf16 v[52:55], v[196:199], v[84:87], v[52:55]
v_mfma_f32_16x16x32_bf16 v[56:59], v[200:203], v[84:87], v[56:59]
v_mfma_f32_16x16x32_bf16 v[60:63], v[204:207], v[84:87], v[60:63]
ds_read_b64_tr_b16 v[192:193], v4 offset:45056
ds_read_b64_tr_b16 v[194:195], v4 offset:47104
ds_read_b64_tr_b16 v[196:197], v5 offset:45056
ds_read_b64_tr_b16 v[198:199], v5 offset:47104
ds_read_b64_tr_b16 v[200:201], v6 offset:45056
ds_read_b64_tr_b16 v[202:203], v6 offset:47104
ds_read_b64_tr_b16 v[204:205], v7 offset:45056
ds_read_b64_tr_b16 v[206:207], v7 offset:47104
s_waitcnt vmcnt(7) lgkmcnt(14)
v_mfma_f32_16x16x32_bf16 v[48:51], v[208:211], v[88:91], v[48:51]
buffer_load_dwordx4 v[120:123], v1, s[16:19], s91 offen offset:2048 sc1
v_mfma_f32_16x16x32_bf16 v[52:55], v[212:215], v[88:91], v[52:55]
v_mfma_f32_16x16x32_bf16 v[56:59], v[216:219], v[88:91], v[56:59]
v_mfma_f32_16x16x32_bf16 v[60:63], v[220:223], v[88:91], v[60:63]
ds_read_b64_tr_b16 v[208:209], v4 offset:49152
ds_read_b64_tr_b16 v[210:211], v4 offset:51200
ds_read_b64_tr_b16 v[212:213], v5 offset:49152
ds_read_b64_tr_b16 v[214:215], v5 offset:51200
ds_read_b64_tr_b16 v[216:217], v6 offset:49152
ds_read_b64_tr_b16 v[218:219], v6 offset:51200
ds_read_b64_tr_b16 v[220:221], v7 offset:49152
ds_read_b64_tr_b16 v[222:223], v7 offset:51200
s_waitcnt vmcnt(7)
v_mfma_f32_16x16x32_bf16 v[48:51], v[224:227], v[92:95], v[48:51]
buffer_load_dwordx4 v[124:127], v1, s[16:19], s95 offen sc1
v_mfma_f32_16x16x32_bf16 v[52:55], v[228:231], v[92:95], v[52:55]
v_mfma_f32_16x16x32_bf16 v[56:59], v[232:235], v[92:95], v[56:59]
v_mfma_f32_16x16x32_bf16 v[60:63], v[236:239], v[92:95], v[60:63]
ds_read_b64_tr_b16 v[224:225], v4 offset:53248
ds_read_b64_tr_b16 v[226:227], v4 offset:55296
ds_read_b64_tr_b16 v[228:229], v5 offset:53248
ds_read_b64_tr_b16 v[230:231], v5 offset:55296
ds_read_b64_tr_b16 v[232:233], v6 offset:53248
ds_read_b64_tr_b16 v[234:235], v6 offset:55296
ds_read_b64_tr_b16 v[236:237], v7 offset:53248
ds_read_b64_tr_b16 v[238:239], v7 offset:55296
s_waitcnt vmcnt(7) lgkmcnt(14)
v_mfma_f32_16x16x32_bf16 v[48:51], v[176:179], v[96:99], v[48:51]
buffer_load_dwordx4 v[128:131], v1, s[16:19], s95 offen offset:1024 sc1
v_mfma_f32_16x16x32_bf16 v[52:55], v[180:183], v[96:99], v[52:55]
v_mfma_f32_16x16x32_bf16 v[56:59], v[184:187], v[96:99], v[56:59]
v_mfma_f32_16x16x32_bf16 v[60:63], v[188:191], v[96:99], v[60:63]
ds_read_b64_tr_b16 v[176:177], v8 offset:0
ds_read_b64_tr_b16 v[178:179], v8 offset:2048
ds_read_b64_tr_b16 v[180:181], v9 offset:0
ds_read_b64_tr_b16 v[182:183], v9 offset:2048
ds_read_b64_tr_b16 v[184:185], v10 offset:0
ds_read_b64_tr_b16 v[186:187], v10 offset:2048
ds_read_b64_tr_b16 v[188:189], v11 offset:0
ds_read_b64_tr_b16 v[190:191], v11 offset:2048
s_waitcnt vmcnt(7)
v_mfma_f32_16x16x32_bf16 v[48:51], v[192:195], v[100:103], v[48:51]
buffer_load_dwordx4 v[132:135], v1, s[16:19], s95 offen offset:2048 sc1
v_mfma_f32_16x16x32_bf16 v[52:55], v[196:199], v[100:103], v[52:55]
v_mfma_f32_16x16x32_bf16 v[56:59], v[200:203], v[100:103], v[56:59]
v_mfma_f32_16x16x32_bf16 v[60:63], v[204:207], v[100:103], v[60:63]
ds_read_b64_tr_b16 v[192:193], v8 offset:4096
ds_read_b64_tr_b16 v[194:195], v8 offset:6144
ds_read_b64_tr_b16 v[196:197], v9 offset:4096
ds_read_b64_tr_b16 v[198:199], v9 offset:6144
ds_read_b64_tr_b16 v[200:201], v10 offset:4096
ds_read_b64_tr_b16 v[202:203], v10 offset:6144
ds_read_b64_tr_b16 v[204:205], v11 offset:4096
ds_read_b64_tr_b16 v[206:207], v11 offset:6144
s_waitcnt vmcnt(7) lgkmcnt(14)
v_mfma_f32_16x16x32_bf16 v[48:51], v[208:211], v[104:107], v[48:51]
buffer_load_dwordx4 v[136:139], v1, s[16:19], s92 offen sc1
v_mfma_f32_16x16x32_bf16 v[52:55], v[212:215], v[104:107], v[52:55]
v_mfma_f32_16x16x32_bf16 v[56:59], v[216:219], v[104:107], v[56:59]
v_mfma_f32_16x16x32_bf16 v[60:63], v[220:223], v[104:107], v[60:63]
ds_read_b64_tr_b16 v[208:209], v8 offset:8192
ds_read_b64_tr_b16 v[210:211], v8 offset:10240
ds_read_b64_tr_b16 v[212:213], v9 offset:8192
ds_read_b64_tr_b16 v[214:215], v9 offset:10240
ds_read_b64_tr_b16 v[216:217], v10 offset:8192
ds_read_b64_tr_b16 v[218:219], v10 offset:10240
ds_read_b64_tr_b16 v[220:221], v11 offset:8192
ds_read_b64_tr_b16 v[222:223], v11 offset:10240
s_waitcnt vmcnt(7)
v_mfma_f32_16x16x32_bf16 v[48:51], v[224:227], v[108:111], v[48:51]
buffer_load_dwordx4 v[140:143], v1, s[16:19], s92 offen offset:1024 sc1
v_mfma_f32_16x16x32_bf16 v[52:55], v[228:231], v[108:111], v[52:55]
v_mfma_f32_16x16x32_bf16 v[56:59], v[232:235], v[108:111], v[56:59]
v_mfma_f32_16x16x32_bf16 v[60:63], v[236:239], v[108:111], v[60:63]
ds_read_b64_tr_b16 v[224:225], v8 offset:16384
ds_read_b64_tr_b16 v[226:227], v8 offset:18432
ds_read_b64_tr_b16 v[228:229], v9 offset:16384
ds_read_b64_tr_b16 v[230:231], v9 offset:18432
ds_read_b64_tr_b16 v[232:233], v10 offset:16384
ds_read_b64_tr_b16 v[234:235], v10 offset:18432
ds_read_b64_tr_b16 v[236:237], v11 offset:16384
ds_read_b64_tr_b16 v[238:239], v11 offset:18432
s_waitcnt vmcnt(7) lgkmcnt(14)
v_mfma_f32_16x16x32_bf16 v[48:51], v[176:179], v[112:115], v[48:51]
buffer_load_dwordx4 v[144:147], v1, s[16:19], s92 offen offset:2048 sc1
v_mfma_f32_16x16x32_bf16 v[52:55], v[180:183], v[112:115], v[52:55]
v_mfma_f32_16x16x32_bf16 v[56:59], v[184:187], v[112:115], v[56:59]
v_mfma_f32_16x16x32_bf16 v[60:63], v[188:191], v[112:115], v[60:63]
ds_read_b64_tr_b16 v[176:177], v8 offset:20480
ds_read_b64_tr_b16 v[178:179], v8 offset:22528
ds_read_b64_tr_b16 v[180:181], v9 offset:20480
ds_read_b64_tr_b16 v[182:183], v9 offset:22528
ds_read_b64_tr_b16 v[184:185], v10 offset:20480
ds_read_b64_tr_b16 v[186:187], v10 offset:22528
ds_read_b64_tr_b16 v[188:189], v11 offset:20480
ds_read_b64_tr_b16 v[190:191], v11 offset:22528
s_waitcnt vmcnt(7)
v_mfma_f32_16x16x32_bf16 v[48:51], v[192:195], v[116:119], v[48:51]
buffer_load_dwordx4 v[148:151], v1, s[16:19], s96 offen sc1
v_mfma_f32_16x16x32_bf16 v[52:55], v[196:199], v[116:119], v[52:55]
v_mfma_f32_16x16x32_bf16 v[56:59], v[200:203], v[116:119], v[56:59]
v_mfma_f32_16x16x32_bf16 v[60:63], v[204:207], v[116:119], v[60:63]
ds_read_b64_tr_b16 v[192:193], v8 offset:24576
ds_read_b64_tr_b16 v[194:195], v8 offset:26624
ds_read_b64_tr_b16 v[196:197], v9 offset:24576
ds_read_b64_tr_b16 v[198:199], v9 offset:26624
ds_read_b64_tr_b16 v[200:201], v10 offset:24576
ds_read_b64_tr_b16 v[202:203], v10 offset:26624
ds_read_b64_tr_b16 v[204:205], v11 offset:24576
ds_read_b64_tr_b16 v[206:207], v11 offset:26624
s_waitcnt vmcnt(7) lgkmcnt(14)
v_mfma_f32_16x16x32_bf16 v[48:51], v[208:211], v[120:123], v[48:51]
s_mov_b64 exec, 1
buffer_load_dword v2, off, s[16:19], s69 sc1
s_mov_b64 exec, -1
v_mfma_f32_16x16x32_bf16 v[52:55], v[212:215], v[120:123], v[52:55]
v_mfma_f32_16x16x32_bf16 v[56:59], v[216:219], v[120:123], v[56:59]
v_mfma_f32_16x16x32_bf16 v[60:63], v[220:223], v[120:123], v[60:63]
ds_read_b64_tr_b16 v[208:209], v8 offset:28672
ds_read_b64_tr_b16 v[210:211], v8 offset:30720
ds_read_b64_tr_b16 v[212:213], v9 offset:28672
ds_read_b64_tr_b16 v[214:215], v9 offset:30720
ds_read_b64_tr_b16 v[216:217], v10 offset:28672
ds_read_b64_tr_b16 v[218:219], v10 offset:30720
ds_read_b64_tr_b16 v[220:221], v11 offset:28672
ds_read_b64_tr_b16 v[222:223], v11 offset:30720
s_waitcnt vmcnt(7)
v_mfma_f32_16x16x32_bf16 v[48:51], v[224:227], v[124:127], v[48:51]
buffer_load_dwordx4 v[152:155], v1, s[16:19], s96 offen offset:1024 sc1
v_mfma_f32_16x16x32_bf16 v[52:55], v[228:231], v[124:127], v[52:55]
v_mfma_f32_16x16x32_bf16 v[56:59], v[232:235], v[124:127], v[56:59]
v_mfma_f32_16x16x32_bf16 v[60:63], v[236:239], v[124:127], v[60:63]
ds_read_b64_tr_b16 v[224:225], v8 offset:32768
ds_read_b64_tr_b16 v[226:227], v8 offset:34816
ds_read_b64_tr_b16 v[228:229], v9 offset:32768
ds_read_b64_tr_b16 v[230:231], v9 offset:34816
ds_read_b64_tr_b16 v[232:233], v10 offset:32768
ds_read_b64_tr_b16 v[234:235], v10 offset:34816
ds_read_b64_tr_b16 v[236:237], v11 offset:32768
ds_read_b64_tr_b16 v[238:239], v11 offset:34816
s_waitcnt vmcnt(7) lgkmcnt(14)
v_mfma_f32_16x16x32_bf16 v[48:51], v[176:179], v[128:131], v[48:51]
buffer_load_dwordx4 v[156:159], v1, s[16:19], s96 offen offset:2048 sc1
v_mfma_f32_16x16x32_bf16 v[52:55], v[180:183], v[128:131], v[52:55]
v_mfma_f32_16x16x32_bf16 v[56:59], v[184:187], v[128:131], v[56:59]
v_mfma_f32_16x16x32_bf16 v[60:63], v[188:191], v[128:131], v[60:63]
ds_read_b64_tr_b16 v[176:177], v8 offset:36864
ds_read_b64_tr_b16 v[178:179], v8 offset:38912
ds_read_b64_tr_b16 v[180:181], v9 offset:36864
ds_read_b64_tr_b16 v[182:183], v9 offset:38912
ds_read_b64_tr_b16 v[184:185], v10 offset:36864
ds_read_b64_tr_b16 v[186:187], v10 offset:38912
ds_read_b64_tr_b16 v[188:189], v11 offset:36864
ds_read_b64_tr_b16 v[190:191], v11 offset:38912
s_waitcnt vmcnt(7)
v_mfma_f32_16x16x32_bf16 v[48:51], v[192:195], v[132:135], v[48:51]
v_mfma_f32_16x16x32_bf16 v[52:55], v[196:199], v[132:135], v[52:55]
v_mfma_f32_16x16x32_bf16 v[56:59], v[200:203], v[132:135], v[56:59]
v_mfma_f32_16x16x32_bf16 v[60:63], v[204:207], v[132:135], v[60:63]
s_waitcnt vmcnt(2)
v_readfirstlane_b32 s67, v2
s_nop 0
s_cmp_eq_u32 s67, 8
s_cbranch_scc1 .Lk_yok_28
s_mov_b64 exec, 1
s_mov_b32 s66, 32768
.Lk_poll_29:
buffer_load_dword v2, off, s[16:19], s69 sc1
s_waitcnt vmcnt(0)
v_cmp_ne_u32_e32 vcc, 8, v2
s_nop 4
s_cbranch_vccz .Lk_done_30
s_sub_u32 s66, s66, 1
s_cbranch_scc0 .Lk_poll_29
.Lk_done_30:
s_mov_b64 exec, -1
.Lk_yok_28:
buffer_load_dwordx4 v[160:163], v1, s[16:19], s89 offen offset:3072 sc1
buffer_load_dwordx4 v[164:167], v1, s[16:19], s90 offen offset:3072 sc1
buffer_load_dwordx4 v[168:171], v1, s[16:19], s91 offen offset:3072 sc1
buffer_load_dwordx4 v[172:175], v1, s[16:19], s92 offen offset:3072 sc1
ds_read_b64_tr_b16 v[192:193], v8 offset:45056
ds_read_b64_tr_b16 v[194:195], v8 offset:47104
ds_read_b64_tr_b16 v[196:197], v9 offset:45056
ds_read_b64_tr_b16 v[198:199], v9 offset:47104
ds_read_b64_tr_b16 v[200:201], v10 offset:45056
ds_read_b64_tr_b16 v[202:203], v10 offset:47104
ds_read_b64_tr_b16 v[204:205], v11 offset:45056
ds_read_b64_tr_b16 v[206:207], v11 offset:47104
s_waitcnt lgkmcnt(14)
v_mfma_f32_16x16x32_bf16 v[48:51], v[208:211], v[136:139], v[48:51]
v_mfma_f32_16x16x32_bf16 v[52:55], v[212:215], v[136:139], v[52:55]
v_mfma_f32_16x16x32_bf16 v[56:59], v[216:219], v[136:139], v[56:59]
v_mfma_f32_16x16x32_bf16 v[60:63], v[220:223], v[136:139], v[60:63]
ds_read_b64_tr_b16 v[208:209], v8 offset:49152
ds_read_b64_tr_b16 v[210:211], v8 offset:51200
ds_read_b64_tr_b16 v[212:213], v9 offset:49152
ds_read_b64_tr_b16 v[214:215], v9 offset:51200
ds_read_b64_tr_b16 v[216:217], v10 offset:49152
ds_read_b64_tr_b16 v[218:219], v10 offset:51200
ds_read_b64_tr_b16 v[220:221], v11 offset:49152
ds_read_b64_tr_b16 v[222:223], v11 offset:51200
v_mfma_f32_16x16x32_bf16 v[48:51], v[224:227], v[140:143], v[48:51]
v_mfma_f32_16x16x32_bf16 v[52:55], v[228:231], v[140:143], v[52:55]
v_mfma_f32_16x16x32_bf16 v[56:59], v[232:235], v[140:143], v[56:59]
v_mfma_f32_16x16x32_bf16 v[60:63], v[236:239], v[140:143], v[60:63]
ds_read_b64_tr_b16 v[224:225], v8 offset:53248
ds_read_b64_tr_b16 v[226:227], v8 offset:55296
ds_read_b64_tr_b16 v[228:229], v9 offset:53248
ds_read_b64_tr_b16 v[230:231], v9 offset:55296
ds_read_b64_tr_b16 v[232:233], v10 offset:53248
ds_read_b64_tr_b16 v[234:235], v10 offset:55296
ds_read_b64_tr_b16 v[236:237], v11 offset:53248
ds_read_b64_tr_b16 v[238:239], v11 offset:55296
s_waitcnt lgkmcnt(14)
v_mfma_f32_16x16x32_bf16 v[48:51], v[176:179], v[144:147], v[48:51]
v_mfma_f32_16x16x32_bf16 v[52:55], v[180:183], v[144:147], v[52:55]
v_mfma_f32_16x16x32_bf16 v[56:59], v[184:187], v[144:147], v[56:59]
v_mfma_f32_16x16x32_bf16 v[60:63], v[188:191], v[144:147], v[60:63]
ds_read_b64_tr_b16 v[176:177], v4 offset:12288
ds_read_b64_tr_b16 v[178:179], v4 offset:14336
ds_read_b64_tr_b16 v[180:181], v5 offset:12288
ds_read_b64_tr_b16 v[182:183], v5 offset:14336
ds_read_b64_tr_b16 v[184:185], v6 offset:12288
ds_read_b64_tr_b16 v[186:187], v6 offset:14336
ds_read_b64_tr_b16 v[188:189], v7 offset:12288
ds_read_b64_tr_b16 v[190:191], v7 offset:14336
v_mfma_f32_16x16x32_bf16 v[48:51], v[192:195], v[148:151], v[48:51]
v_mfma_f32_16x16x32_bf16 v[52:55], v[196:199], v[148:151], v[52:55]
v_mfma_f32_16x16x32_bf16 v[56:59], v[200:203], v[148:151], v[56:59]
v_mfma_f32_16x16x32_bf16 v[60:63], v[204:207], v[148:151], v[60:63]
ds_read_b64_tr_b16 v[192:193], v4 offset:40960
ds_read_b64_tr_b16 v[194:195], v4 offset:43008
ds_read_b64_tr_b16 v[196:197], v5 offset:40960
ds_read_b64_tr_b16 v[198:199], v5 offset:43008
ds_read_b64_tr_b16 v[200:201], v6 offset:40960
ds_read_b64_tr_b16 v[202:203], v6 offset:43008
ds_read_b64_tr_b16 v[204:205], v7 offset:40960
ds_read_b64_tr_b16 v[206:207], v7 offset:43008
s_waitcnt vmcnt(5) lgkmcnt(14)
v_mfma_f32_16x16x32_bf16 v[48:51], v[208:211], v[152:155], v[48:51]
v_mfma_f32_16x16x32_bf16 v[52:55], v[212:215], v[152:155], v[52:55]
v_mfma_f32_16x16x32_bf16 v[56:59], v[216:219], v[152:155], v[56:59]
v_mfma_f32_16x16x32_bf16 v[60:63], v[220:223], v[152:155], v[60:63]
ds_read_b64_tr_b16 v[208:209], v8 offset:12288
ds_read_b64_tr_b16 v[210:211], v8 offset:14336
ds_read_b64_tr_b16 v[212:213], v9 offset:12288
ds_read_b64_tr_b16 v[214:215], v9 offset:14336
ds_read_b64_tr_b16 v[216:217], v10 offset:12288
ds_read_b64_tr_b16 v[218:219], v10 offset:14336
ds_read_b64_tr_b16 v[220:221], v11 offset:12288
ds_read_b64_tr_b16 v[222:223], v11 offset:14336
s_waitcnt vmcnt(4)
v_mfma_f32_16x16x32_bf16 v[48:51], v[224:227], v[156:159], v[48:51]
v_mfma_f32_16x16x32_bf16 v[52:55], v[228:231], v[156:159], v[52:55]
v_mfma_f32_16x16x32_bf16 v[56:59], v[232:235], v[156:159], v[56:59]
v_mfma_f32_16x16x32_bf16 v[60:63], v[236:239], v[156:159], v[60:63]
ds_read_b64_tr_b16 v[224:225], v8 offset:40960
ds_read_b64_tr_b16 v[226:227], v8 offset:43008
ds_read_b64_tr_b16 v[228:229], v9 offset:40960
ds_read_b64_tr_b16 v[230:231], v9 offset:43008
ds_read_b64_tr_b16 v[232:233], v10 offset:40960
ds_read_b64_tr_b16 v[234:235], v10 offset:43008
ds_read_b64_tr_b16 v[236:237], v11 offset:40960
ds_read_b64_tr_b16 v[238:239], v11 offset:43008
s_waitcnt vmcnt(3) lgkmcnt(14)
v_mfma_f32_16x16x32_bf16 v[48:51], v[176:179], v[160:163], v[48:51]
v_mfma_f32_16x16x32_bf16 v[52:55], v[180:183], v[160:163], v[52:55]
v_mfma_f32_16x16x32_bf16 v[56:59], v[184:187], v[160:163], v[56:59]
v_mfma_f32_16x16x32_bf16 v[60:63], v[188:191], v[160:163], v[60:63]
s_waitcnt vmcnt(2)
v_mfma_f32_16x16x32_bf16 v[48:51], v[192:195], v[164:167], v[48:51]
v_mfma_f32_16x16x32_bf16 v[52:55], v[196:199], v[164:167], v[52:55]
v_mfma_f32_16x16x32_bf16 v[56:59], v[200:203], v[164:167], v[56:59]
v_mfma_f32_16x16x32_bf16 v[60:63], v[204:207], v[164:167], v[60:63]
s_waitcnt vmcnt(1) lgkmcnt(8)
v_mfma_f32_16x16x32_bf16 v[48:51], v[208:211], v[168:171], v[48:51]
v_mfma_f32_16x16x32_bf16 v[52:55], v[212:215], v[168:171], v[52:55]
v_mfma_f32_16x16x32_bf16 v[56:59], v[216:219], v[168:171], v[56:59]
v_mfma_f32_16x16x32_bf16 v[60:63], v[220:223], v[168:171], v[60:63]
s_waitcnt vmcnt(0) lgkmcnt(0)
v_mfma_f32_16x16x32_bf16 v[48:51], v[224:227], v[172:175], v[48:51]
v_mfma_f32_16x16x32_bf16 v[52:55], v[228:231], v[172:175], v[52:55]
v_mfma_f32_16x16x32_bf16 v[56:59], v[232:235], v[172:175], v[56:59]
v_mfma_f32_16x16x32_bf16 v[60:63], v[236:239], v[172:175], v[60:63]
s_nop 4
v_pk_add_f32 v[240:241], v[240:241], v[48:49]
v_pk_add_f32 v[242:243], v[242:243], v[50:51]
v_pk_add_f32 v[244:245], v[244:245], v[52:53]
v_pk_add_f32 v[246:247], v[246:247], v[54:55]
v_pk_add_f32 v[248:249], v[248:249], v[56:57]
v_pk_add_f32 v[250:251], v[250:251], v[58:59]
v_pk_add_f32 v[252:253], v[252:253], v[60:61]
v_pk_add_f32 v[254:255], v[254:255], v[62:63]
v_cvt_pk_bf16_f32 v24, v240, v241
v_cvt_pk_bf16_f32 v25, v242, v243
v_cvt_pk_bf16_f32 v26, v244, v245
v_cvt_pk_bf16_f32 v27, v246, v247
v_cvt_pk_bf16_f32 v28, v248, v249
v_cvt_pk_bf16_f32 v29, v250, v251
v_cvt_pk_bf16_f32 v30, v252, v253
v_cvt_pk_bf16_f32 v31, v254, v255
ds_write_b64 v16, v[24:25] offset:0
ds_write_b64 v17, v[26:27] offset:0
ds_write_b64 v18, v[28:29] offset:0
ds_write_b64 v19, v[30:31] offset:0
ds_read_b128 v[240:243], v14 offset:0
ds_read_b128 v[244:247], v14 offset:1024
s_waitcnt lgkmcnt(1)
buffer_store_dwordx4 v[240:243], v15, s[40:43], s71 offen sc1
s_waitcnt lgkmcnt(0)
buffer_store_dwordx4 v[244:247], v15, s[40:43], s72 offen sc1
.Lk_fin:
.Lk_end:
)ASM"
               :
               : "s"(__builtin_amdgcn_kernarg_segment_ptr()), "s"(blockIdx.x), "v"(threadIdx.x), "s"((unsigned)(size_t)lds)
               : "s3", "s4", "s5", "s6", "s7", "s8", "s9", "s10", "s11", "s12", "s13", "s14", "s15", "s16", "s17", "s18", "s19", "s20", "s21", "s22", "s23", "s24", "s25", "s26", "s27", "s28", "s29", "s30", "s31", "s36", "s37", "s38", "s39", "s40", "s41", "s42", "s43", "s44", "s45", "s46", "s47", "s48", "s49", "s50", "s51", "s52", "s53", "s54", "s55", "s56", "s57", "s58", "s59", "s60", "s61", "s62", "s63", "s64", "s65", "s66", "s67", "s68", "s69", "s70", "s71", "s72", "s73", "s74", "s75", "s76", "s77", "s78", "s79", "s80", "s81", "s82", "s83", "s84", "s85", "s86", "s87", "s88", "s89", "s90", "s91", "s92", "s93", "s94", "s95", "s96", "s97", "s98", "s99", "v1", "v2", "v3", "v4", "v5", "v6", "v7", "v8", "v9", "v10", "v11", "v12", "v13", "v14", "v15", "v16", "v17", "v18", "v19", "v20", "v21", "v22", "v23", "v24", "v25", "v26", "v27", "v28", "v29", "v30", "v31", "v32", "v33", "v34", "v35", "v36", "v37", "v38", "v39", "v40", "v41", "v42", "v43", "v44", "v45", "v46", "v47", "v48", "v49", "v50", "v51", "v52", "v53", "v54", "v55", "v56", "v57", "v58", "v59", "v60", "v61", "v62", "v63", "v64", "v65", "v66", "v67", "v68", "v69", "v70", "v71", "v72", "v73", "v74", "v75", "v76", "v77", "v78", "v79", "v80", "v81", "v82", "v83", "v84", "v85", "v86", "v87", "v88", "v89", "v90", "v91", "v92", "v93", "v94", "v95", "v96", "v97", "v98", "v99", "v100", "v101", "v102", "v103", "v104", "v105", "v106", "v107", "v108", "v109", "v110", "v111", "v112", "v113", "v114", "v115", "v116", "v117", "v118", "v119", "v120", "v121", "v122", "v123", "v124", "v125", "v126", "v127", "v128", "v129", "v130", "v131", "v132", "v133", "v134", "v135", "v136", "v137", "v138", "v139", "v140", "v141", "v142", "v143", "v144", "v145", "v146", "v147", "v148", "v149", "v150", "v151", "v152", "v153", "v154", "v155", "v156", "v157", "v158", "v159", "v160", "v161", "v162", "v163", "v164", "v165", "v166", "v167", "v168", "v169", "v170", "v171", "v172", "v173", "v174", "v175", "v176", "v177", "v178", "v179", "v180", "v181", "v182", "v183", "v184", "v185", "v186", "v187", "v188", "v189", "v190", "v191", "v192", "v193", "v194", "v195", "v196", "v197", "v198", "v199", "v200", "v201", "v202", "v203", "v204", "v205", "v206", "v207", "v208", "v209", "v210", "v211", "v212", "v213", "v214", "v215", "v216", "v217", "v218", "v219", "v220", "v221", "v222", "v223", "v224", "v225", "v226", "v227", "v228", "v229", "v230", "v231", "v232", "v233", "v234", "v235", "v236", "v237", "v238", "v239", "v240", "v241", "v242", "v243", "v244", "v245", "v246", "v247", "v248", "v249", "v250", "v251", "v252", "v253", "v254", "v255", "a0", "a1", "a2", "a3", "a4", "a5", "a6", "a7", "a8", "a9", "a10", "a11", "a12", "a13", "a14", "a15", "a16", "a17", "a18", "a19", "a20", "a21", "a22", "a23", "a24", "a25", "a26", "a27", "a28", "a29", "a30", "a31", "a32", "a33", "a34", "a35", "a36", "a37", "a38", "a39", "a40", "a41", "a42", "a43", "a44", "a45", "a46", "a47", "a48", "a49", "a50", "a51", "a52", "a53", "a54", "a55", "a56", "a57", "a58", "a59", "a60", "a61", "a62", "a63", "a64", "a65", "a66", "a67", "a68", "a69", "a70", "a71", "a72", "a73", "a74", "a75", "a76", "a77", "a78", "a79", "a80", "a81", "a82", "a83", "a84", "a85", "a86", "a87", "a88", "a89", "a90", "a91", "a92", "a93", "a94", "a95", "a96", "a97", "a98", "a99", "a100", "a101", "a102", "a103", "a104", "a105", "a106", "a107", "a108", "a109", "a110", "a111", "a112", "a113", "a114", "a115", "a116", "a117", "a118", "a119", "a120", "a121", "a122", "a123", "a124", "a125", "a126", "a127", "a128", "a129", "a130", "a131", "a132", "a133", "a134", "a135", "a136", "a137", "a138", "a139", "a140", "a141", "a142", "a143", "a144", "a145", "a146", "a147", "a148", "a149", "a150", "a151", "a152", "a153", "a154", "a155", "a156", "a157", "a158", "a159", "a160", "a161", "a162", "a163", "a164", "a165", "a166", "a167", "a168", "a169", "a170", "a171", "a172", "a173", "a174", "a175", "a176", "a177", "a178", "a179", "a180", "a181", "a182", "a183", "a184", "a185", "a186", "a187", "a188", "a189", "a190", "a191", "a192", "a193", "a194", "a195", "a196", "a197", "a198", "a199", "a200", "a201", "a202", "a203", "a204", "a205", "a206", "a207", "a208", "a209", "a210", "a211", "a212", "a213", "a214", "a215", "a216", "a217", "a218", "a219", "a220", "a221", "a222", "a223", "a224", "a225", "a226", "a227", "a228", "a229", "a230", "a231", "a232", "a233", "a234", "a235", "a236", "a237", "a238", "a239", "a240", "a241", "a242", "a243", "a244", "a245", "a246", "a247", "a248", "a249", "a250", "a251", "a252", "a253", "a254", "a255", "vcc", "scc", "memory");
}

extern "C" void kernel_launch(void* const* d_in, const int* in_sizes, int n_in, void* d_out, int out_size, void* d_ws,
                              size_t ws_size, hipStream_t stream) {
  fused_block_kernel<<<256, 256, 0, stream>>>(d_in[0], d_in[1], d_ws, d_in[2], d_in[4], d_in[5], d_in[7], d_out, d_in[6]);
}
```
